```python
import torch

def _softmax(x, axis=-1):
    return torch.softmax(x, dim=axis)
def _gelu(x):
    return torch.nn.functional.gelu(x, approximate="tanh")
def _silu(x):
    return torch.nn.functional.silu(x)
def _sigmoid(x):
    return torch.sigmoid(x)
def _layernorm(x, axis=-1, eps=1e-5):
    m = x.mean(dim=axis, keepdim=True)
    v = x.var(dim=axis, keepdim=True, unbiased=False)
    return (x - m) / torch.sqrt(v + eps)
def _scatter_add(tgt, idx, src, axis=0):
    return tgt.clone().index_add_(axis, idx, src)
def _segment_sum(src, seg, n):
    out = torch.zeros((n,) + src.shape[1:], dtype=src.dtype)
    return out.index_add_(0, seg, src)
def _pad(x, pw):
    flat = tuple(v for pair in reversed(pw) for v in pair)
    return torch.nn.functional.pad(x, flat)

kernel_name = 'parallel_sum__softmax_full_mha__glu_gated_swiglu__prompt'

def setup_inputs():

    _w = (lambda a: (lambda t: t.to(torch.bfloat16) if t.is_floating_point() else t)(torch.as_tensor(a)))
    def _A_setup():
        B, S, D = 1, 2048, 640
        return {
            "x": _w((lambda *s: torch.randn(*s, dtype=torch.bfloat16) * 0.05)(B, S, D)),
            "wqkv": _w((lambda *s: torch.randn(*s, dtype=torch.bfloat16) * 0.05)(D, 3 * D)),
            "wo": _w((lambda *s: torch.randn(*s, dtype=torch.bfloat16) * 0.05)(D, D)),
        }
    _A_in = _A_setup()

    def _B_setup():
        B, S, D, F = 1, 2048, 640, 1536
        return {
            "x": _w((lambda *s: torch.randn(*s, dtype=torch.bfloat16) * 0.05)(B, S, D)),
            "w1": _w((lambda *s: torch.randn(*s, dtype=torch.bfloat16) * 0.4)(D, F) * (2.0 / D) ** 0.5),
            "w3": _w((lambda *s: torch.randn(*s, dtype=torch.bfloat16) * 0.4)(D, F) * (2.0 / D) ** 0.5),
            "w2": _w((lambda *s: torch.randn(*s, dtype=torch.bfloat16) * 0.4)(F, D) * (2.0 / F) ** 0.5),
        }
    _B_in = _B_setup()

    return {
        "A_x": _A_in["x"],
        "A_wqkv": _A_in["wqkv"],
        "A_wo": _A_in["wo"],
        "B_x": _B_in["x"],
        "B_w1": _B_in["w1"],
        "B_w3": _B_in["w3"],
        "B_w2": _B_in["w2"],
    }

def reference(A_x, A_wqkv, A_wo, B_x, B_w1, B_w3, B_w2):
    def _A_ref(x, wqkv, wo):
        B, S, D = x.shape
        H = 32; Dh = D // H
        qkv = (lambda x, *d: x.permute(*d))((x @ wqkv).reshape(B, S, 3, H, Dh), 2, 0, 3, 1, 4)
        q, k, v = qkv[0], qkv[1], qkv[2]
        scale = Dh ** -0.5
        scores = torch.einsum("bhqd,bhkd->bhqk", q, k) * scale
        p = _softmax(scores, axis=-1)
        o = torch.einsum("bhqk,bhkd->bhqd", p, v)
        return (lambda x, *d: x.permute(*d))(o, 0, 2, 1, 3).reshape(B, S, D) @ wo

    def _B_ref(x, w1, w3, w2):
        gate = torch.einsum("bsd,df->bsf", x, w1)
        up = torch.einsum("bsd,df->bsf", x, w3)
        h = _silu(gate) * up
        out = torch.einsum("bsf,fd->bsd", h, w2)
        return out

    x = A_x
    xn = _layernorm(x, axis=-1)
    return x + _A_ref(xn, A_wqkv, A_wo) + _B_ref(xn, B_w1, B_w3, B_w2)
```

```cpp
#include <hip/hip_runtime.h>
#include <hip/hip_bf16.h>
typedef __hip_bfloat16 bf16;
constexpr int S = 2048, D = 640, F = 1536, H = 32, DH = 20;
__device__ inline float b2f(bf16 x) { return __bfloat162float(x); }
__global__ void ln_k(const bf16* x, bf16* xn) {
    int row = blockIdx.x; __shared__ float red[256];
    float s = 0; for (int i = threadIdx.x; i < D; i += 256) s += b2f(x[row * D + i]);
    red[threadIdx.x] = s; __syncthreads();
    for (int o = 128; o > 0; o >>= 1) { if (threadIdx.x < o) red[threadIdx.x] += red[threadIdx.x + o]; __syncthreads(); }
    float m = red[0] / D; __syncthreads();
    s = 0; for (int i = threadIdx.x; i < D; i += 256) { float d = b2f(x[row * D + i]) - m; s += d * d; }
    red[threadIdx.x] = s; __syncthreads();
    for (int o = 128; o > 0; o >>= 1) { if (threadIdx.x < o) red[threadIdx.x] += red[threadIdx.x + o]; __syncthreads(); }
    float r = rsqrtf(red[0] / D + 1e-5f);
    for (int i = threadIdx.x; i < D; i += 256) xn[row * D + i] = __float2bfloat16((b2f(x[row * D + i]) - m) * r);
}
__global__ void gemm_k(const bf16* A, const bf16* W, float* C, int M, int N, int K) {
    int n = blockIdx.x * 256 + threadIdx.x, m = blockIdx.y; if (n >= N) return;
    float s = 0; for (int k = 0; k < K; ++k) s += b2f(A[m * K + k]) * b2f(W[k * N + n]);
    C[m * N + n] = s;
}
__global__ void attn_k(const float* qkv, bf16* o) {
    int q = blockIdx.x * 64 + threadIdx.x, h = blockIdx.y;
    float qv[DH], acc[DH]; for (int d = 0; d < DH; ++d) { qv[d] = b2f(__float2bfloat16(qkv[q * 1920 + h * DH + d])) * 0.22360679775f; acc[d] = 0; }
    float m = -1e30f, l = 0;
    for (int k = 0; k < S; ++k) {
        float s = 0; for (int d = 0; d < DH; ++d) s += qv[d] * b2f(__float2bfloat16(qkv[k * 1920 + 640 + h * DH + d]));
        float mn = fmaxf(m, s), a = __expf(m - mn), p = __expf(s - mn); l = l * a + p;
        for (int d = 0; d < DH; ++d) acc[d] = acc[d] * a + p * b2f(__float2bfloat16(qkv[k * 1920 + 1280 + h * DH + d]));
        m = mn;
    }
    for (int d = 0; d < DH; ++d) o[q * D + h * DH + d] = __float2bfloat16(acc[d] / l);
}
__global__ void swiglu_k(const float* g, const float* u, bf16* h) {
    int i = blockIdx.x * 256 + threadIdx.x; float a = g[i]; h[i] = __float2bfloat16(a / (1.f + __expf(-a)) * u[i]);
}
__global__ void final_k(const bf16* x, const float* a, const float* b, bf16* out) {
    int i = blockIdx.x * 256 + threadIdx.x; out[i] = __float2bfloat16(b2f(x[i]) + a[i] + b[i]);
}
extern "C" void kernel_launch(void* const* d_in, const int* in_sizes, int n_in, void* d_out, int out_size, void* d_ws, size_t ws_size, hipStream_t stream) {
    const bf16 *x = (const bf16*)d_in[0], *wqkv = (const bf16*)d_in[1], *wo = (const bf16*)d_in[2], *w1 = (const bf16*)d_in[4], *w3 = (const bf16*)d_in[5], *w2 = (const bf16*)d_in[6];
    char* ws = (char*)d_ws;
    bf16* xn = (bf16*)ws; float* qkv = (float*)(ws + (4 << 20)); float* g = (float*)(ws + (24 << 20)); float* u = (float*)(ws + (40 << 20));
    bf16* o = (bf16*)(ws + (56 << 20)); bf16* h = (bf16*)(ws + (60 << 20)); float* a = (float*)(ws + (70 << 20)); float* b = (float*)(ws + (80 << 20));
    ln_k<<<S, 256, 0, stream>>>(x, xn);
    gemm_k<<<dim3(8, S), 256, 0, stream>>>(xn, wqkv, qkv, S, 1920, D);
    gemm_k<<<dim3(6, S), 256, 0, stream>>>(xn, w1, g, S, F, D);
    gemm_k<<<dim3(6, S), 256, 0, stream>>>(xn, w3, u, S, F, D);
    attn_k<<<dim3(S / 64, H), 64, 0, stream>>>(qkv, o);
    swiglu_k<<<S * F / 256, 256, 0, stream>>>(g, u, h);
    gemm_k<<<dim3(3, S), 256, 0, stream>>>(o, wo, a, S, D, D);
    gemm_k<<<dim3(3, S), 256, 0, stream>>>(h, w2, b, S, D, F);
    final_k<<<S * D / 256, 256, 0, stream>>>(x, a, b, (bf16*)d_out);
}
```

```cpp
#include <hip/hip_runtime.h>

typedef unsigned long long u64;
typedef float f32x4 __attribute__((ext_vector_type(4)));
typedef float f32x8 __attribute__((ext_vector_type(8)));
typedef float f32x16 __attribute__((ext_vector_type(16)));
typedef float f32x32 __attribute__((ext_vector_type(32)));
typedef float f32x2 __attribute__((ext_vector_type(2)));
typedef unsigned u32x2 __attribute__((ext_vector_type(2)));
typedef unsigned u32x4 __attribute__((ext_vector_type(4)));
typedef unsigned u32x8 __attribute__((ext_vector_type(8)));
typedef __bf16 bf16x2 __attribute__((ext_vector_type(2)));
typedef __bf16 bf16x8 __attribute__((ext_vector_type(8)));
typedef __bf16 bf16x16 __attribute__((ext_vector_type(16)));
typedef short s16x4 __attribute__((ext_vector_type(4)));
#define LAS __attribute__((address_space(3)))
#define BARRIER() asm volatile("s_waitcnt lgkmcnt(0)\n\ts_barrier" ::: "memory")

constexpr int S = 2048, D = 640, F = 1536;
constexpr size_t WS_XN = 0;
constexpr size_t WS_W2T = WS_XN + 128 * 20 * 1024;
constexpr size_t WS_WOT = WS_W2T + 48 * 40 * 1024;
constexpr size_t WS_Q = WS_WOT + 20 * 40 * 1024;
constexpr size_t WS_KV = WS_Q + 32 * 64 * 1280;
constexpr size_t WS_H = WS_KV + 32 * 64 * 2560;
constexpr size_t WS_O = WS_H + 128 * 48 * 1024;
constexpr size_t WS_OT = WS_O + 128 * 16 * 1024;
constexpr size_t WS_FLAG = WS_OT + 32 * 2048 * 8;
constexpr int F_XN = 0, F_H = 256, F_QKV = 512, F_O = F_QKV + 2048, F_RT = F_O + 2048, F_END = F_RT + 4 * 136;
constexpr size_t WS_EPOCH = WS_FLAG + F_END * 128;

#define STAMP(id)
#define CSTAMP(id, c)
#define FLUSH(mask)
#define DGARG
#define DGDECL
__device__ __forceinline__ unsigned pk2(float lo, float hi) {
  f32x2 v = {lo, hi};
  return __builtin_bit_cast(unsigned, __builtin_convertvector(v, bf16x2));
}
__device__ __forceinline__ float bflo(unsigned u) { return __builtin_bit_cast(float, u << 16); }
__device__ __forceinline__ float bfhi(unsigned u) { return __builtin_bit_cast(float, u & 0xffff0000u); }
__device__ __forceinline__ float rbf(float x) { return bflo(pk2(x, x)); }
__device__ __forceinline__ f32x4 mfma16(u32x4 a, u32x4 b, f32x4 c) {
  return __builtin_amdgcn_mfma_f32_16x16x32_bf16(__builtin_bit_cast(bf16x8, a), __builtin_bit_cast(bf16x8, b), c, 0, 0, 0);
}

#define ST_AUX 16
typedef __amdgpu_buffer_rsrc_t rsrc_t;
__device__ __forceinline__ rsrc_t ws_rsrc(const char* ws) { return __builtin_amdgcn_make_buffer_rsrc(const_cast<char*>(ws), 0, 0x4000000, 0x00020000); }
__device__ __forceinline__ void st16(rsrc_t r, unsigned off, u32x4 v) { __builtin_amdgcn_raw_buffer_store_b128(v, r, off, 0, ST_AUX); }
__device__ __forceinline__ void st8(rsrc_t r, unsigned off, u32x2 v) { __builtin_amdgcn_raw_buffer_store_b64(v, r, off, 0, ST_AUX); }
__device__ __forceinline__ void swap32(unsigned& a, unsigned& b) { auto t = __builtin_amdgcn_permlane32_swap(a, b, false, false); a = t[0]; b = t[1]; }
__device__ __forceinline__ void swap32f(float& a, float& b) {
  unsigned x = __builtin_bit_cast(unsigned, a), y = __builtin_bit_cast(unsigned, b);
  swap32(x, y); a = __builtin_bit_cast(float, x); b = __builtin_bit_cast(float, y);
}

constexpr int LDS_KV = 40960, LDS_HI = 102400, LDS_LN = 158720, LDS_TAG = 159232, LDS_CNT = 159296, LDS_BYTES = 163840;

__device__ __forceinline__ void k1_main(u32x8 ha, unsigned wv, unsigned xa, rsrc_t rs, unsigned ob, unsigned o2) {
  unsigned so; const u64 sc = 0x3ea52b5c3ea52b5cull, nl = 0xbfb8aa3bbfb8aa3bull;
  asm volatile(
    "v_add_u32 v158, 61440, v153\n\t"
    "v_add_u32 v159, 61440, v154\n\t"
    "v_add_u32 v160, 61440, v155\n\t"
    "v_add_u32 v161, 61440, v156\n\t"
    "v_add_u32 v162, 61440, v157\n\t"
    "s_cmp_eq_u32 %[xa], 2\n\t"
    "s_cbranch_scc1 Lxa_%=\n\t"
    "s_barrier\n\t"
    "ds_read_b64_tr_b16 v[96:97], v153 offset:0\n\t"
    "ds_read_b64_tr_b16 v[98:99], v153 offset:512\n\t"
    "ds_read_b128 v[80:83], v152 offset:0\n\t"
    "ds_read_b128 v[84:87], v152 offset:1024\n\t"
    "ds_read_b128 v[88:91], v152 offset:2048\n\t"
    "ds_read_b128 v[92:95], v152 offset:3072\n\t"
    "ds_read_b64_tr_b16 v[100:101], v154 offset:0\n\t"
    "ds_read_b64_tr_b16 v[102:103], v154 offset:512\n\t"
    "ds_read_b64_tr_b16 v[104:105], v155 offset:0\n\t"
    "ds_read_b64_tr_b16 v[106:107], v155 offset:512\n\t"
    "ds_read_b64_tr_b16 v[108:109], v156 offset:0\n\t"
    "ds_read_b64_tr_b16 v[110:111], v156 offset:512\n\t"
    "ds_read_b64_tr_b16 v[112:113], v157 offset:0\n\t"
    "ds_read_b64_tr_b16 v[114:115], v157 offset:512\n\t"
    "s_waitcnt lgkmcnt(0)\n\t"
    "v_mfma_f32_16x16x32_bf16 v[0:3], v[96:99], v[80:83], 0\n\t"
    "v_mfma_f32_16x16x32_bf16 v[20:23], v[96:99], v[84:87], 0\n\t"
    "s_barrier\n\t"
    "v_mfma_f32_16x16x32_bf16 v[40:43], v[96:99], v[88:91], 0\n\t"
    "ds_read_b64_tr_b16 v[132:133], v153 offset:20480\n\t"
    "v_mfma_f32_16x16x32_bf16 v[60:63], v[96:99], v[92:95], 0\n\t"
    "ds_read_b64_tr_b16 v[134:135], v153 offset:20992\n\t"
    "v_mfma_f32_16x16x32_bf16 v[64:67], v[100:103], v[92:95], 0\n\t"
    "ds_read_b128 v[116:119], v152 offset:8192\n\t"
    "v_mfma_f32_16x16x32_bf16 v[44:47], v[100:103], v[88:91], 0\n\t"
    "ds_read_b128 v[120:123], v152 offset:9216\n\t"
    "v_mfma_f32_16x16x32_bf16 v[24:27], v[100:103], v[84:87], 0\n\t"
    "ds_read_b128 v[124:127], v152 offset:10240\n\t"
    "v_mfma_f32_16x16x32_bf16 v[4:7], v[100:103], v[80:83], 0\n\t"
    "ds_read_b128 v[128:131], v152 offset:11264\n\t"
    "v_mfma_f32_16x16x32_bf16 v[8:11], v[104:107], v[80:83], 0\n\t"
    "ds_read_b64_tr_b16 v[136:137], v154 offset:20480\n\t"
    "v_mfma_f32_16x16x32_bf16 v[28:31], v[104:107], v[84:87], 0\n\t"
    "ds_read_b64_tr_b16 v[138:139], v154 offset:20992\n\t"
    "v_mfma_f32_16x16x32_bf16 v[48:51], v[104:107], v[88:91], 0\n\t"
    "ds_read_b64_tr_b16 v[140:141], v155 offset:20480\n\t"
    "v_mfma_f32_16x16x32_bf16 v[68:71], v[104:107], v[92:95], 0\n\t"
    "ds_read_b64_tr_b16 v[142:143], v155 offset:20992\n\t"
    "v_mfma_f32_16x16x32_bf16 v[72:75], v[108:111], v[92:95], 0\n\t"
    "ds_read_b64_tr_b16 v[144:145], v156 offset:20480\n\t"
    "v_mfma_f32_16x16x32_bf16 v[52:55], v[108:111], v[88:91], 0\n\t"
    "ds_read_b64_tr_b16 v[146:147], v156 offset:20992\n\t"
    "v_mfma_f32_16x16x32_bf16 v[32:35], v[108:111], v[84:87], 0\n\t"
    "ds_read_b64_tr_b16 v[148:149], v157 offset:20480\n\t"
    "v_mfma_f32_16x16x32_bf16 v[12:15], v[108:111], v[80:83], 0\n\t"
    "ds_read_b64_tr_b16 v[150:151], v157 offset:20992\n\t"
    "v_mfma_f32_16x16x32_bf16 v[16:19], v[112:115], v[80:83], 0\n\t"
    "v_mfma_f32_16x16x32_bf16 v[36:39], v[112:115], v[84:87], 0\n\t"
    "v_mfma_f32_16x16x32_bf16 v[56:59], v[112:115], v[88:91], 0\n\t"
    "v_mfma_f32_16x16x32_bf16 v[76:79], v[112:115], v[92:95], 0\n\t"
    "s_waitcnt lgkmcnt(0)\n\t"
    "v_mfma_f32_16x16x32_bf16 v[0:3], v[132:135], v[116:119], v[0:3]\n\t"
    "v_mfma_f32_16x16x32_bf16 v[20:23], v[132:135], v[120:123], v[20:23]\n\t"
    "s_barrier\n\t"
    "v_mfma_f32_16x16x32_bf16 v[40:43], v[132:135], v[124:127], v[40:43]\n\t"
    "ds_read_b64_tr_b16 v[96:97], v153 offset:40960\n\t"
    "v_mfma_f32_16x16x32_bf16 v[60:63], v[132:135], v[128:131], v[60:63]\n\t"
    "ds_read_b64_tr_b16 v[98:99], v153 offset:41472\n\t"
    "v_mfma_f32_16x16x32_bf16 v[64:67], v[136:139], v[128:131], v[64:67]\n\t"
    "ds_read_b128 v[80:83], v152 offset:16384\n\t"
    "v_mfma_f32_16x16x32_bf16 v[44:47], v[136:139], v[124:127], v[44:47]\n\t"
    "ds_read_b128 v[84:87], v152 offset:17408\n\t"
    "v_mfma_f32_16x16x32_bf16 v[24:27], v[136:139], v[120:123], v[24:27]\n\t"
    "ds_read_b128 v[88:91], v152 offset:18432\n\t"
    "v_mfma_f32_16x16x32_bf16 v[4:7], v[136:139], v[116:119], v[4:7]\n\t"
    "ds_read_b128 v[92:95], v152 offset:19456\n\t"
    "v_mfma_f32_16x16x32_bf16 v[8:11], v[140:143], v[116:119], v[8:11]\n\t"
    "ds_read_b64_tr_b16 v[100:101], v154 offset:40960\n\t"
    "v_mfma_f32_16x16x32_bf16 v[28:31], v[140:143], v[120:123], v[28:31]\n\t"
    "ds_read_b64_tr_b16 v[102:103], v154 offset:41472\n\t"
    "v_mfma_f32_16x16x32_bf16 v[48:51], v[140:143], v[124:127], v[48:51]\n\t"
    "ds_read_b64_tr_b16 v[104:105], v155 offset:40960\n\t"
    "v_mfma_f32_16x16x32_bf16 v[68:71], v[140:143], v[128:131], v[68:71]\n\t"
    "ds_read_b64_tr_b16 v[106:107], v155 offset:41472\n\t"
    "v_mfma_f32_16x16x32_bf16 v[72:75], v[144:147], v[128:131], v[72:75]\n\t"
    "ds_read_b64_tr_b16 v[108:109], v156 offset:40960\n\t"
    "v_mfma_f32_16x16x32_bf16 v[52:55], v[144:147], v[124:127], v[52:55]\n\t"
    "ds_read_b64_tr_b16 v[110:111], v156 offset:41472\n\t"
    "v_mfma_f32_16x16x32_bf16 v[32:35], v[144:147], v[120:123], v[32:35]\n\t"
    "ds_read_b64_tr_b16 v[112:113], v157 offset:40960\n\t"
    "v_mfma_f32_16x16x32_bf16 v[12:15], v[144:147], v[116:119], v[12:15]\n\t"
    "ds_read_b64_tr_b16 v[114:115], v157 offset:41472\n\t"
    "v_mfma_f32_16x16x32_bf16 v[16:19], v[148:151], v[116:119], v[16:19]\n\t"
    "v_mfma_f32_16x16x32_bf16 v[36:39], v[148:151], v[120:123], v[36:39]\n\t"
    "v_mfma_f32_16x16x32_bf16 v[56:59], v[148:151], v[124:127], v[56:59]\n\t"
    "v_mfma_f32_16x16x32_bf16 v[76:79], v[148:151], v[128:131], v[76:79]\n\t"
    "s_waitcnt lgkmcnt(0)\n\t"
    "v_mfma_f32_16x16x32_bf16 v[0:3], v[96:99], v[80:83], v[0:3]\n\t"
    "v_mfma_f32_16x16x32_bf16 v[20:23], v[96:99], v[84:87], v[20:23]\n\t"
    "s_barrier\n\t"
    "v_mfma_f32_16x16x32_bf16 v[40:43], v[96:99], v[88:91], v[40:43]\n\t"
    "ds_read_b64_tr_b16 v[132:133], v158 offset:0\n\t"
    "v_mfma_f32_16x16x32_bf16 v[60:63], v[96:99], v[92:95], v[60:63]\n\t"
    "ds_read_b64_tr_b16 v[134:135], v158 offset:512\n\t"
    "v_mfma_f32_16x16x32_bf16 v[64:67], v[100:103], v[92:95], v[64:67]\n\t"
    "ds_read_b128 v[116:119], v152 offset:24576\n\t"
    "v_mfma_f32_16x16x32_bf16 v[44:47], v[100:103], v[88:91], v[44:47]\n\t"
    "ds_read_b128 v[120:123], v152 offset:25600\n\t"
    "v_mfma_f32_16x16x32_bf16 v[24:27], v[100:103], v[84:87], v[24:27]\n\t"
    "ds_read_b128 v[124:127], v152 offset:26624\n\t"
    "v_mfma_f32_16x16x32_bf16 v[4:7], v[100:103], v[80:83], v[4:7]\n\t"
    "ds_read_b128 v[128:131], v152 offset:27648\n\t"
    "v_mfma_f32_16x16x32_bf16 v[8:11], v[104:107], v[80:83], v[8:11]\n\t"
    "ds_read_b64_tr_b16 v[136:137], v159 offset:0\n\t"
    "v_mfma_f32_16x16x32_bf16 v[28:31], v[104:107], v[84:87], v[28:31]\n\t"
    "ds_read_b64_tr_b16 v[138:139], v159 offset:512\n\t"
    "v_mfma_f32_16x16x32_bf16 v[48:51], v[104:107], v[88:91], v[48:51]\n\t"
    "ds_read_b64_tr_b16 v[140:141], v160 offset:0\n\t"
    "v_mfma_f32_16x16x32_bf16 v[68:71], v[104:107], v[92:95], v[68:71]\n\t"
    "ds_read_b64_tr_b16 v[142:143], v160 offset:512\n\t"
    "v_mfma_f32_16x16x32_bf16 v[72:75], v[108:111], v[92:95], v[72:75]\n\t"
    "ds_read_b64_tr_b16 v[144:145], v161 offset:0\n\t"
    "v_mfma_f32_16x16x32_bf16 v[52:55], v[108:111], v[88:91], v[52:55]\n\t"
    "ds_read_b64_tr_b16 v[146:147], v161 offset:512\n\t"
    "v_mfma_f32_16x16x32_bf16 v[32:35], v[108:111], v[84:87], v[32:35]\n\t"
    "ds_read_b64_tr_b16 v[148:149], v162 offset:0\n\t"
    "v_mfma_f32_16x16x32_bf16 v[12:15], v[108:111], v[80:83], v[12:15]\n\t"
    "ds_read_b64_tr_b16 v[150:151], v162 offset:512\n\t"
    "v_mfma_f32_16x16x32_bf16 v[16:19], v[112:115], v[80:83], v[16:19]\n\t"
    "v_mfma_f32_16x16x32_bf16 v[36:39], v[112:115], v[84:87], v[36:39]\n\t"
    "v_mfma_f32_16x16x32_bf16 v[56:59], v[112:115], v[88:91], v[56:59]\n\t"
    "v_mfma_f32_16x16x32_bf16 v[76:79], v[112:115], v[92:95], v[76:79]\n\t"
    "s_waitcnt lgkmcnt(0)\n\t"
    "v_mfma_f32_16x16x32_bf16 v[0:3], v[132:135], v[116:119], v[0:3]\n\t"
    "v_mfma_f32_16x16x32_bf16 v[20:23], v[132:135], v[120:123], v[20:23]\n\t"
    "s_barrier\n\t"
    "v_mfma_f32_16x16x32_bf16 v[40:43], v[132:135], v[124:127], v[40:43]\n\t"
    "ds_read_b64_tr_b16 v[96:97], v158 offset:20480\n\t"
    "v_mfma_f32_16x16x32_bf16 v[60:63], v[132:135], v[128:131], v[60:63]\n\t"
    "ds_read_b64_tr_b16 v[98:99], v158 offset:20992\n\t"
    "v_mfma_f32_16x16x32_bf16 v[64:67], v[136:139], v[128:131], v[64:67]\n\t"
    "ds_read_b128 v[80:83], v152 offset:32768\n\t"
    "v_mfma_f32_16x16x32_bf16 v[44:47], v[136:139], v[124:127], v[44:47]\n\t"
    "ds_read_b128 v[84:87], v152 offset:33792\n\t"
    "v_mfma_f32_16x16x32_bf16 v[24:27], v[136:139], v[120:123], v[24:27]\n\t"
    "ds_read_b128 v[88:91], v152 offset:34816\n\t"
    "v_mfma_f32_16x16x32_bf16 v[4:7], v[136:139], v[116:119], v[4:7]\n\t"
    "ds_read_b128 v[92:95], v152 offset:35840\n\t"
    "v_mfma_f32_16x16x32_bf16 v[8:11], v[140:143], v[116:119], v[8:11]\n\t"
    "ds_read_b64_tr_b16 v[100:101], v159 offset:20480\n\t"
    "v_mfma_f32_16x16x32_bf16 v[28:31], v[140:143], v[120:123], v[28:31]\n\t"
    "ds_read_b64_tr_b16 v[102:103], v159 offset:20992\n\t"
    "v_mfma_f32_16x16x32_bf16 v[48:51], v[140:143], v[124:127], v[48:51]\n\t"
    "ds_read_b64_tr_b16 v[104:105], v160 offset:20480\n\t"
    "v_mfma_f32_16x16x32_bf16 v[68:71], v[140:143], v[128:131], v[68:71]\n\t"
    "ds_read_b64_tr_b16 v[106:107], v160 offset:20992\n\t"
    "v_mfma_f32_16x16x32_bf16 v[72:75], v[144:147], v[128:131], v[72:75]\n\t"
    "ds_read_b64_tr_b16 v[108:109], v161 offset:20480\n\t"
    "v_mfma_f32_16x16x32_bf16 v[52:55], v[144:147], v[124:127], v[52:55]\n\t"
    "ds_read_b64_tr_b16 v[110:111], v161 offset:20992\n\t"
    "v_mfma_f32_16x16x32_bf16 v[32:35], v[144:147], v[120:123], v[32:35]\n\t"
    "ds_read_b64_tr_b16 v[112:113], v162 offset:20480\n\t"
    "v_mfma_f32_16x16x32_bf16 v[12:15], v[144:147], v[116:119], v[12:15]\n\t"
    "ds_read_b64_tr_b16 v[114:115], v162 offset:20992\n\t"
    "v_mfma_f32_16x16x32_bf16 v[16:19], v[148:151], v[116:119], v[16:19]\n\t"
    "v_mfma_f32_16x16x32_bf16 v[36:39], v[148:151], v[120:123], v[36:39]\n\t"
    "v_mfma_f32_16x16x32_bf16 v[56:59], v[148:151], v[124:127], v[56:59]\n\t"
    "v_mfma_f32_16x16x32_bf16 v[76:79], v[148:151], v[128:131], v[76:79]\n\t"
    "s_waitcnt lgkmcnt(0)\n\t"
    "v_mfma_f32_16x16x32_bf16 v[0:3], v[96:99], v[80:83], v[0:3]\n\t"
    "v_mfma_f32_16x16x32_bf16 v[20:23], v[96:99], v[84:87], v[20:23]\n\t"
    "s_barrier\n\t"
    "v_mfma_f32_16x16x32_bf16 v[40:43], v[96:99], v[88:91], v[40:43]\n\t"
    "ds_read_b64_tr_b16 v[132:133], v153 offset:0\n\t"
    "v_mfma_f32_16x16x32_bf16 v[60:63], v[96:99], v[92:95], v[60:63]\n\t"
    "ds_read_b64_tr_b16 v[134:135], v153 offset:512\n\t"
    "v_mfma_f32_16x16x32_bf16 v[64:67], v[100:103], v[92:95], v[64:67]\n\t"
    "ds_read_b128 v[116:119], v152 offset:0\n\t"
    "v_mfma_f32_16x16x32_bf16 v[44:47], v[100:103], v[88:91], v[44:47]\n\t"
    "ds_read_b128 v[120:123], v152 offset:1024\n\t"
    "v_mfma_f32_16x16x32_bf16 v[24:27], v[100:103], v[84:87], v[24:27]\n\t"
    "ds_read_b128 v[124:127], v152 offset:2048\n\t"
    "v_mfma_f32_16x16x32_bf16 v[4:7], v[100:103], v[80:83], v[4:7]\n\t"
    "ds_read_b128 v[128:131], v152 offset:3072\n\t"
    "v_mfma_f32_16x16x32_bf16 v[8:11], v[104:107], v[80:83], v[8:11]\n\t"
    "ds_read_b64_tr_b16 v[136:137], v154 offset:0\n\t"
    "v_mfma_f32_16x16x32_bf16 v[28:31], v[104:107], v[84:87], v[28:31]\n\t"
    "ds_read_b64_tr_b16 v[138:139], v154 offset:512\n\t"
    "v_mfma_f32_16x16x32_bf16 v[48:51], v[104:107], v[88:91], v[48:51]\n\t"
    "ds_read_b64_tr_b16 v[140:141], v155 offset:0\n\t"
    "v_mfma_f32_16x16x32_bf16 v[68:71], v[104:107], v[92:95], v[68:71]\n\t"
    "ds_read_b64_tr_b16 v[142:143], v155 offset:512\n\t"
    "v_mfma_f32_16x16x32_bf16 v[72:75], v[108:111], v[92:95], v[72:75]\n\t"
    "ds_read_b64_tr_b16 v[144:145], v156 offset:0\n\t"
    "v_mfma_f32_16x16x32_bf16 v[52:55], v[108:111], v[88:91], v[52:55]\n\t"
    "ds_read_b64_tr_b16 v[146:147], v156 offset:512\n\t"
    "v_mfma_f32_16x16x32_bf16 v[32:35], v[108:111], v[84:87], v[32:35]\n\t"
    "ds_read_b64_tr_b16 v[148:149], v157 offset:0\n\t"
    "v_mfma_f32_16x16x32_bf16 v[12:15], v[108:111], v[80:83], v[12:15]\n\t"
    "ds_read_b64_tr_b16 v[150:151], v157 offset:512\n\t"
    "v_mfma_f32_16x16x32_bf16 v[16:19], v[112:115], v[80:83], v[16:19]\n\t"
    "v_mfma_f32_16x16x32_bf16 v[36:39], v[112:115], v[84:87], v[36:39]\n\t"
    "v_mfma_f32_16x16x32_bf16 v[56:59], v[112:115], v[88:91], v[56:59]\n\t"
    "v_mfma_f32_16x16x32_bf16 v[76:79], v[112:115], v[92:95], v[76:79]\n\t"
    "s_waitcnt lgkmcnt(0)\n\t"
    "v_mfma_f32_16x16x32_bf16 v[0:3], v[132:135], v[116:119], v[0:3]\n\t"
    "v_mfma_f32_16x16x32_bf16 v[20:23], v[132:135], v[120:123], v[20:23]\n\t"
    "s_barrier\n\t"
    "v_mfma_f32_16x16x32_bf16 v[40:43], v[132:135], v[124:127], v[40:43]\n\t"
    "ds_read_b64_tr_b16 v[96:97], v153 offset:20480\n\t"
    "v_mfma_f32_16x16x32_bf16 v[60:63], v[132:135], v[128:131], v[60:63]\n\t"
    "ds_read_b64_tr_b16 v[98:99], v153 offset:20992\n\t"
    "v_mfma_f32_16x16x32_bf16 v[64:67], v[136:139], v[128:131], v[64:67]\n\t"
    "ds_read_b128 v[80:83], v152 offset:8192\n\t"
    "v_mfma_f32_16x16x32_bf16 v[44:47], v[136:139], v[124:127], v[44:47]\n\t"
    "ds_read_b128 v[84:87], v152 offset:9216\n\t"
    "v_mfma_f32_16x16x32_bf16 v[24:27], v[136:139], v[120:123], v[24:27]\n\t"
    "ds_read_b128 v[88:91], v152 offset:10240\n\t"
    "v_mfma_f32_16x16x32_bf16 v[4:7], v[136:139], v[116:119], v[4:7]\n\t"
    "ds_read_b128 v[92:95], v152 offset:11264\n\t"
    "v_mfma_f32_16x16x32_bf16 v[8:11], v[140:143], v[116:119], v[8:11]\n\t"
    "ds_read_b64_tr_b16 v[100:101], v154 offset:20480\n\t"
    "v_mfma_f32_16x16x32_bf16 v[28:31], v[140:143], v[120:123], v[28:31]\n\t"
    "ds_read_b64_tr_b16 v[102:103], v154 offset:20992\n\t"
    "v_mfma_f32_16x16x32_bf16 v[48:51], v[140:143], v[124:127], v[48:51]\n\t"
    "ds_read_b64_tr_b16 v[104:105], v155 offset:20480\n\t"
    "v_mfma_f32_16x16x32_bf16 v[68:71], v[140:143], v[128:131], v[68:71]\n\t"
    "ds_read_b64_tr_b16 v[106:107], v155 offset:20992\n\t"
    "v_mfma_f32_16x16x32_bf16 v[72:75], v[144:147], v[128:131], v[72:75]\n\t"
    "ds_read_b64_tr_b16 v[108:109], v156 offset:20480\n\t"
    "v_mfma_f32_16x16x32_bf16 v[52:55], v[144:147], v[124:127], v[52:55]\n\t"
    "ds_read_b64_tr_b16 v[110:111], v156 offset:20992\n\t"
    "v_mfma_f32_16x16x32_bf16 v[32:35], v[144:147], v[120:123], v[32:35]\n\t"
    "ds_read_b64_tr_b16 v[112:113], v157 offset:20480\n\t"
    "v_mfma_f32_16x16x32_bf16 v[12:15], v[144:147], v[116:119], v[12:15]\n\t"
    "ds_read_b64_tr_b16 v[114:115], v157 offset:20992\n\t"
    "v_mfma_f32_16x16x32_bf16 v[16:19], v[148:151], v[116:119], v[16:19]\n\t"
    "v_mfma_f32_16x16x32_bf16 v[36:39], v[148:151], v[120:123], v[36:39]\n\t"
    "v_mfma_f32_16x16x32_bf16 v[56:59], v[148:151], v[124:127], v[56:59]\n\t"
    "v_mfma_f32_16x16x32_bf16 v[76:79], v[148:151], v[128:131], v[76:79]\n\t"
    "s_waitcnt lgkmcnt(0)\n\t"
    "v_mfma_f32_16x16x32_bf16 v[0:3], v[96:99], v[80:83], v[0:3]\n\t"
    "v_mfma_f32_16x16x32_bf16 v[20:23], v[96:99], v[84:87], v[20:23]\n\t"
    "s_barrier\n\t"
    "v_mfma_f32_16x16x32_bf16 v[40:43], v[96:99], v[88:91], v[40:43]\n\t"
    "ds_read_b64_tr_b16 v[132:133], v153 offset:40960\n\t"
    "v_mfma_f32_16x16x32_bf16 v[60:63], v[96:99], v[92:95], v[60:63]\n\t"
    "ds_read_b64_tr_b16 v[134:135], v153 offset:41472\n\t"
    "v_mfma_f32_16x16x32_bf16 v[64:67], v[100:103], v[92:95], v[64:67]\n\t"
    "ds_read_b128 v[116:119], v152 offset:16384\n\t"
    "v_mfma_f32_16x16x32_bf16 v[44:47], v[100:103], v[88:91], v[44:47]\n\t"
    "ds_read_b128 v[120:123], v152 offset:17408\n\t"
    "v_mfma_f32_16x16x32_bf16 v[24:27], v[100:103], v[84:87], v[24:27]\n\t"
    "ds_read_b128 v[124:127], v152 offset:18432\n\t"
    "v_mfma_f32_16x16x32_bf16 v[4:7], v[100:103], v[80:83], v[4:7]\n\t"
    "ds_read_b128 v[128:131], v152 offset:19456\n\t"
    "v_mfma_f32_16x16x32_bf16 v[8:11], v[104:107], v[80:83], v[8:11]\n\t"
    "ds_read_b64_tr_b16 v[136:137], v154 offset:40960\n\t"
    "v_mfma_f32_16x16x32_bf16 v[28:31], v[104:107], v[84:87], v[28:31]\n\t"
    "ds_read_b64_tr_b16 v[138:139], v154 offset:41472\n\t"
    "v_mfma_f32_16x16x32_bf16 v[48:51], v[104:107], v[88:91], v[48:51]\n\t"
    "ds_read_b64_tr_b16 v[140:141], v155 offset:40960\n\t"
    "v_mfma_f32_16x16x32_bf16 v[68:71], v[104:107], v[92:95], v[68:71]\n\t"
    "ds_read_b64_tr_b16 v[142:143], v155 offset:41472\n\t"
    "v_mfma_f32_16x16x32_bf16 v[72:75], v[108:111], v[92:95], v[72:75]\n\t"
    "ds_read_b64_tr_b16 v[144:145], v156 offset:40960\n\t"
    "v_mfma_f32_16x16x32_bf16 v[52:55], v[108:111], v[88:91], v[52:55]\n\t"
    "ds_read_b64_tr_b16 v[146:147], v156 offset:41472\n\t"
    "v_mfma_f32_16x16x32_bf16 v[32:35], v[108:111], v[84:87], v[32:35]\n\t"
    "ds_read_b64_tr_b16 v[148:149], v157 offset:40960\n\t"
    "v_mfma_f32_16x16x32_bf16 v[12:15], v[108:111], v[80:83], v[12:15]\n\t"
    "ds_read_b64_tr_b16 v[150:151], v157 offset:41472\n\t"
    "v_mfma_f32_16x16x32_bf16 v[16:19], v[112:115], v[80:83], v[16:19]\n\t"
    "v_mfma_f32_16x16x32_bf16 v[36:39], v[112:115], v[84:87], v[36:39]\n\t"
    "v_mfma_f32_16x16x32_bf16 v[56:59], v[112:115], v[88:91], v[56:59]\n\t"
    "v_mfma_f32_16x16x32_bf16 v[76:79], v[112:115], v[92:95], v[76:79]\n\t"
    "s_waitcnt lgkmcnt(0)\n\t"
    "v_mfma_f32_16x16x32_bf16 v[0:3], v[132:135], v[116:119], v[0:3]\n\t"
    "v_mfma_f32_16x16x32_bf16 v[20:23], v[132:135], v[120:123], v[20:23]\n\t"
    "s_barrier\n\t"
    "v_mfma_f32_16x16x32_bf16 v[40:43], v[132:135], v[124:127], v[40:43]\n\t"
    "ds_read_b64_tr_b16 v[96:97], v158 offset:0\n\t"
    "v_mfma_f32_16x16x32_bf16 v[60:63], v[132:135], v[128:131], v[60:63]\n\t"
    "ds_read_b64_tr_b16 v[98:99], v158 offset:512\n\t"
    "v_mfma_f32_16x16x32_bf16 v[64:67], v[136:139], v[128:131], v[64:67]\n\t"
    "ds_read_b128 v[80:83], v152 offset:24576\n\t"
    "v_mfma_f32_16x16x32_bf16 v[44:47], v[136:139], v[124:127], v[44:47]\n\t"
    "ds_read_b128 v[84:87], v152 offset:25600\n\t"
    "v_mfma_f32_16x16x32_bf16 v[24:27], v[136:139], v[120:123], v[24:27]\n\t"
    "ds_read_b128 v[88:91], v152 offset:26624\n\t"
    "v_mfma_f32_16x16x32_bf16 v[4:7], v[136:139], v[116:119], v[4:7]\n\t"
    "ds_read_b128 v[92:95], v152 offset:27648\n\t"
    "v_mfma_f32_16x16x32_bf16 v[8:11], v[140:143], v[116:119], v[8:11]\n\t"
    "ds_read_b64_tr_b16 v[100:101], v159 offset:0\n\t"
    "v_mfma_f32_16x16x32_bf16 v[28:31], v[140:143], v[120:123], v[28:31]\n\t"
    "ds_read_b64_tr_b16 v[102:103], v159 offset:512\n\t"
    "v_mfma_f32_16x16x32_bf16 v[48:51], v[140:143], v[124:127], v[48:51]\n\t"
    "ds_read_b64_tr_b16 v[104:105], v160 offset:0\n\t"
    "v_mfma_f32_16x16x32_bf16 v[68:71], v[140:143], v[128:131], v[68:71]\n\t"
    "ds_read_b64_tr_b16 v[106:107], v160 offset:512\n\t"
    "v_mfma_f32_16x16x32_bf16 v[72:75], v[144:147], v[128:131], v[72:75]\n\t"
    "ds_read_b64_tr_b16 v[108:109], v161 offset:0\n\t"
    "v_mfma_f32_16x16x32_bf16 v[52:55], v[144:147], v[124:127], v[52:55]\n\t"
    "ds_read_b64_tr_b16 v[110:111], v161 offset:512\n\t"
    "v_mfma_f32_16x16x32_bf16 v[32:35], v[144:147], v[120:123], v[32:35]\n\t"
    "ds_read_b64_tr_b16 v[112:113], v162 offset:0\n\t"
    "v_mfma_f32_16x16x32_bf16 v[12:15], v[144:147], v[116:119], v[12:15]\n\t"
    "ds_read_b64_tr_b16 v[114:115], v162 offset:512\n\t"
    "v_mfma_f32_16x16x32_bf16 v[16:19], v[148:151], v[116:119], v[16:19]\n\t"
    "v_mfma_f32_16x16x32_bf16 v[36:39], v[148:151], v[120:123], v[36:39]\n\t"
    "v_mfma_f32_16x16x32_bf16 v[56:59], v[148:151], v[124:127], v[56:59]\n\t"
    "v_mfma_f32_16x16x32_bf16 v[76:79], v[148:151], v[128:131], v[76:79]\n\t"
    "s_waitcnt lgkmcnt(0)\n\t"
    "v_mfma_f32_16x16x32_bf16 v[0:3], v[96:99], v[80:83], v[0:3]\n\t"
    "v_mfma_f32_16x16x32_bf16 v[20:23], v[96:99], v[84:87], v[20:23]\n\t"
    "s_barrier\n\t"
    "v_mfma_f32_16x16x32_bf16 v[40:43], v[96:99], v[88:91], v[40:43]\n\t"
    "ds_read_b64_tr_b16 v[132:133], v158 offset:20480\n\t"
    "v_mfma_f32_16x16x32_bf16 v[60:63], v[96:99], v[92:95], v[60:63]\n\t"
    "ds_read_b64_tr_b16 v[134:135], v158 offset:20992\n\t"
    "v_mfma_f32_16x16x32_bf16 v[64:67], v[100:103], v[92:95], v[64:67]\n\t"
    "ds_read_b128 v[116:119], v152 offset:32768\n\t"
    "v_mfma_f32_16x16x32_bf16 v[44:47], v[100:103], v[88:91], v[44:47]\n\t"
    "ds_read_b128 v[120:123], v152 offset:33792\n\t"
    "v_mfma_f32_16x16x32_bf16 v[24:27], v[100:103], v[84:87], v[24:27]\n\t"
    "ds_read_b128 v[124:127], v152 offset:34816\n\t"
    "v_mfma_f32_16x16x32_bf16 v[4:7], v[100:103], v[80:83], v[4:7]\n\t"
    "ds_read_b128 v[128:131], v152 offset:35840\n\t"
    "v_mfma_f32_16x16x32_bf16 v[8:11], v[104:107], v[80:83], v[8:11]\n\t"
    "ds_read_b64_tr_b16 v[136:137], v159 offset:20480\n\t"
    "v_mfma_f32_16x16x32_bf16 v[28:31], v[104:107], v[84:87], v[28:31]\n\t"
    "ds_read_b64_tr_b16 v[138:139], v159 offset:20992\n\t"
    "v_mfma_f32_16x16x32_bf16 v[48:51], v[104:107], v[88:91], v[48:51]\n\t"
    "ds_read_b64_tr_b16 v[140:141], v160 offset:20480\n\t"
    "v_mfma_f32_16x16x32_bf16 v[68:71], v[104:107], v[92:95], v[68:71]\n\t"
    "ds_read_b64_tr_b16 v[142:143], v160 offset:20992\n\t"
    "v_mfma_f32_16x16x32_bf16 v[72:75], v[108:111], v[92:95], v[72:75]\n\t"
    "ds_read_b64_tr_b16 v[144:145], v161 offset:20480\n\t"
    "v_mfma_f32_16x16x32_bf16 v[52:55], v[108:111], v[88:91], v[52:55]\n\t"
    "ds_read_b64_tr_b16 v[146:147], v161 offset:20992\n\t"
    "v_mfma_f32_16x16x32_bf16 v[32:35], v[108:111], v[84:87], v[32:35]\n\t"
    "ds_read_b64_tr_b16 v[148:149], v162 offset:20480\n\t"
    "v_mfma_f32_16x16x32_bf16 v[12:15], v[108:111], v[80:83], v[12:15]\n\t"
    "ds_read_b64_tr_b16 v[150:151], v162 offset:20992\n\t"
    "v_mfma_f32_16x16x32_bf16 v[16:19], v[112:115], v[80:83], v[16:19]\n\t"
    "v_mfma_f32_16x16x32_bf16 v[36:39], v[112:115], v[84:87], v[36:39]\n\t"
    "v_mfma_f32_16x16x32_bf16 v[56:59], v[112:115], v[88:91], v[56:59]\n\t"
    "v_mfma_f32_16x16x32_bf16 v[76:79], v[112:115], v[92:95], v[76:79]\n\t"
    "s_waitcnt lgkmcnt(0)\n\t"
    "v_mfma_f32_16x16x32_bf16 v[0:3], v[132:135], v[116:119], v[0:3]\n\t"
    "v_mfma_f32_16x16x32_bf16 v[20:23], v[132:135], v[120:123], v[20:23]\n\t"
    "s_barrier\n\t"
    "v_mfma_f32_16x16x32_bf16 v[40:43], v[132:135], v[124:127], v[40:43]\n\t"
    "ds_read_b64_tr_b16 v[96:97], v153 offset:0\n\t"
    "v_mfma_f32_16x16x32_bf16 v[60:63], v[132:135], v[128:131], v[60:63]\n\t"
    "ds_read_b64_tr_b16 v[98:99], v153 offset:512\n\t"
    "v_mfma_f32_16x16x32_bf16 v[64:67], v[136:139], v[128:131], v[64:67]\n\t"
    "ds_read_b128 v[80:83], v152 offset:0\n\t"
    "v_mfma_f32_16x16x32_bf16 v[44:47], v[136:139], v[124:127], v[44:47]\n\t"
    "ds_read_b128 v[84:87], v152 offset:1024\n\t"
    "v_mfma_f32_16x16x32_bf16 v[24:27], v[136:139], v[120:123], v[24:27]\n\t"
    "ds_read_b128 v[88:91], v152 offset:2048\n\t"
    "v_mfma_f32_16x16x32_bf16 v[4:7], v[136:139], v[116:119], v[4:7]\n\t"
    "ds_read_b128 v[92:95], v152 offset:3072\n\t"
    "v_mfma_f32_16x16x32_bf16 v[8:11], v[140:143], v[116:119], v[8:11]\n\t"
    "ds_read_b64_tr_b16 v[100:101], v154 offset:0\n\t"
    "v_mfma_f32_16x16x32_bf16 v[28:31], v[140:143], v[120:123], v[28:31]\n\t"
    "ds_read_b64_tr_b16 v[102:103], v154 offset:512\n\t"
    "v_mfma_f32_16x16x32_bf16 v[48:51], v[140:143], v[124:127], v[48:51]\n\t"
    "ds_read_b64_tr_b16 v[104:105], v155 offset:0\n\t"
    "v_mfma_f32_16x16x32_bf16 v[68:71], v[140:143], v[128:131], v[68:71]\n\t"
    "ds_read_b64_tr_b16 v[106:107], v155 offset:512\n\t"
    "v_mfma_f32_16x16x32_bf16 v[72:75], v[144:147], v[128:131], v[72:75]\n\t"
    "ds_read_b64_tr_b16 v[108:109], v156 offset:0\n\t"
    "v_mfma_f32_16x16x32_bf16 v[52:55], v[144:147], v[124:127], v[52:55]\n\t"
    "ds_read_b64_tr_b16 v[110:111], v156 offset:512\n\t"
    "v_mfma_f32_16x16x32_bf16 v[32:35], v[144:147], v[120:123], v[32:35]\n\t"
    "ds_read_b64_tr_b16 v[112:113], v157 offset:0\n\t"
    "v_mfma_f32_16x16x32_bf16 v[12:15], v[144:147], v[116:119], v[12:15]\n\t"
    "ds_read_b64_tr_b16 v[114:115], v157 offset:512\n\t"
    "v_mfma_f32_16x16x32_bf16 v[16:19], v[148:151], v[116:119], v[16:19]\n\t"
    "v_mfma_f32_16x16x32_bf16 v[36:39], v[148:151], v[120:123], v[36:39]\n\t"
    "v_mfma_f32_16x16x32_bf16 v[56:59], v[148:151], v[124:127], v[56:59]\n\t"
    "v_mfma_f32_16x16x32_bf16 v[76:79], v[148:151], v[128:131], v[76:79]\n\t"
    "s_waitcnt lgkmcnt(0)\n\t"
    "v_mfma_f32_16x16x32_bf16 v[0:3], v[96:99], v[80:83], v[0:3]\n\t"
    "v_mfma_f32_16x16x32_bf16 v[20:23], v[96:99], v[84:87], v[20:23]\n\t"
    "s_barrier\n\t"
    "v_mfma_f32_16x16x32_bf16 v[40:43], v[96:99], v[88:91], v[40:43]\n\t"
    "ds_read_b64_tr_b16 v[132:133], v153 offset:20480\n\t"
    "v_mfma_f32_16x16x32_bf16 v[60:63], v[96:99], v[92:95], v[60:63]\n\t"
    "ds_read_b64_tr_b16 v[134:135], v153 offset:20992\n\t"
    "v_mfma_f32_16x16x32_bf16 v[64:67], v[100:103], v[92:95], v[64:67]\n\t"
    "ds_read_b128 v[116:119], v152 offset:8192\n\t"
    "v_mfma_f32_16x16x32_bf16 v[44:47], v[100:103], v[88:91], v[44:47]\n\t"
    "ds_read_b128 v[120:123], v152 offset:9216\n\t"
    "v_mfma_f32_16x16x32_bf16 v[24:27], v[100:103], v[84:87], v[24:27]\n\t"
    "ds_read_b128 v[124:127], v152 offset:10240\n\t"
    "v_mfma_f32_16x16x32_bf16 v[4:7], v[100:103], v[80:83], v[4:7]\n\t"
    "ds_read_b128 v[128:131], v152 offset:11264\n\t"
    "v_mfma_f32_16x16x32_bf16 v[8:11], v[104:107], v[80:83], v[8:11]\n\t"
    "ds_read_b64_tr_b16 v[136:137], v154 offset:20480\n\t"
    "v_mfma_f32_16x16x32_bf16 v[28:31], v[104:107], v[84:87], v[28:31]\n\t"
    "ds_read_b64_tr_b16 v[138:139], v154 offset:20992\n\t"
    "v_mfma_f32_16x16x32_bf16 v[48:51], v[104:107], v[88:91], v[48:51]\n\t"
    "ds_read_b64_tr_b16 v[140:141], v155 offset:20480\n\t"
    "v_mfma_f32_16x16x32_bf16 v[68:71], v[104:107], v[92:95], v[68:71]\n\t"
    "ds_read_b64_tr_b16 v[142:143], v155 offset:20992\n\t"
    "v_mfma_f32_16x16x32_bf16 v[72:75], v[108:111], v[92:95], v[72:75]\n\t"
    "ds_read_b64_tr_b16 v[144:145], v156 offset:20480\n\t"
    "v_mfma_f32_16x16x32_bf16 v[52:55], v[108:111], v[88:91], v[52:55]\n\t"
    "ds_read_b64_tr_b16 v[146:147], v156 offset:20992\n\t"
    "v_mfma_f32_16x16x32_bf16 v[32:35], v[108:111], v[84:87], v[32:35]\n\t"
    "ds_read_b64_tr_b16 v[148:149], v157 offset:20480\n\t"
    "v_mfma_f32_16x16x32_bf16 v[12:15], v[108:111], v[80:83], v[12:15]\n\t"
    "ds_read_b64_tr_b16 v[150:151], v157 offset:20992\n\t"
    "v_mfma_f32_16x16x32_bf16 v[16:19], v[112:115], v[80:83], v[16:19]\n\t"
    "v_mfma_f32_16x16x32_bf16 v[36:39], v[112:115], v[84:87], v[36:39]\n\t"
    "v_mfma_f32_16x16x32_bf16 v[56:59], v[112:115], v[88:91], v[56:59]\n\t"
    "v_mfma_f32_16x16x32_bf16 v[76:79], v[112:115], v[92:95], v[76:79]\n\t"
    "s_waitcnt lgkmcnt(0)\n\t"
    "v_mfma_f32_16x16x32_bf16 v[0:3], v[132:135], v[116:119], v[0:3]\n\t"
    "v_mfma_f32_16x16x32_bf16 v[20:23], v[132:135], v[120:123], v[20:23]\n\t"
    "s_barrier\n\t"
    "v_mfma_f32_16x16x32_bf16 v[40:43], v[132:135], v[124:127], v[40:43]\n\t"
    "ds_read_b64_tr_b16 v[96:97], v153 offset:40960\n\t"
    "v_mfma_f32_16x16x32_bf16 v[60:63], v[132:135], v[128:131], v[60:63]\n\t"
    "ds_read_b64_tr_b16 v[98:99], v153 offset:41472\n\t"
    "v_mfma_f32_16x16x32_bf16 v[64:67], v[136:139], v[128:131], v[64:67]\n\t"
    "ds_read_b128 v[80:83], v152 offset:16384\n\t"
    "v_mfma_f32_16x16x32_bf16 v[44:47], v[136:139], v[124:127], v[44:47]\n\t"
    "ds_read_b128 v[84:87], v152 offset:17408\n\t"
    "v_mfma_f32_16x16x32_bf16 v[24:27], v[136:139], v[120:123], v[24:27]\n\t"
    "ds_read_b128 v[88:91], v152 offset:18432\n\t"
    "v_mfma_f32_16x16x32_bf16 v[4:7], v[136:139], v[116:119], v[4:7]\n\t"
    "ds_read_b128 v[92:95], v152 offset:19456\n\t"
    "v_mfma_f32_16x16x32_bf16 v[8:11], v[140:143], v[116:119], v[8:11]\n\t"
    "ds_read_b64_tr_b16 v[100:101], v154 offset:40960\n\t"
    "v_mfma_f32_16x16x32_bf16 v[28:31], v[140:143], v[120:123], v[28:31]\n\t"
    "ds_read_b64_tr_b16 v[102:103], v154 offset:41472\n\t"
    "v_mfma_f32_16x16x32_bf16 v[48:51], v[140:143], v[124:127], v[48:51]\n\t"
    "ds_read_b64_tr_b16 v[104:105], v155 offset:40960\n\t"
    "v_mfma_f32_16x16x32_bf16 v[68:71], v[140:143], v[128:131], v[68:71]\n\t"
    "ds_read_b64_tr_b16 v[106:107], v155 offset:41472\n\t"
    "v_mfma_f32_16x16x32_bf16 v[72:75], v[144:147], v[128:131], v[72:75]\n\t"
    "ds_read_b64_tr_b16 v[108:109], v156 offset:40960\n\t"
    "v_mfma_f32_16x16x32_bf16 v[52:55], v[144:147], v[124:127], v[52:55]\n\t"
    "ds_read_b64_tr_b16 v[110:111], v156 offset:41472\n\t"
    "v_mfma_f32_16x16x32_bf16 v[32:35], v[144:147], v[120:123], v[32:35]\n\t"
    "ds_read_b64_tr_b16 v[112:113], v157 offset:40960\n\t"
    "v_mfma_f32_16x16x32_bf16 v[12:15], v[144:147], v[116:119], v[12:15]\n\t"
    "ds_read_b64_tr_b16 v[114:115], v157 offset:41472\n\t"
    "v_mfma_f32_16x16x32_bf16 v[16:19], v[148:151], v[116:119], v[16:19]\n\t"
    "v_mfma_f32_16x16x32_bf16 v[36:39], v[148:151], v[120:123], v[36:39]\n\t"
    "v_mfma_f32_16x16x32_bf16 v[56:59], v[148:151], v[124:127], v[56:59]\n\t"
    "v_mfma_f32_16x16x32_bf16 v[76:79], v[148:151], v[128:131], v[76:79]\n\t"
    "s_waitcnt lgkmcnt(0)\n\t"
    "v_mfma_f32_16x16x32_bf16 v[0:3], v[96:99], v[80:83], v[0:3]\n\t"
    "v_mfma_f32_16x16x32_bf16 v[20:23], v[96:99], v[84:87], v[20:23]\n\t"
    "s_barrier\n\t"
    "v_mfma_f32_16x16x32_bf16 v[40:43], v[96:99], v[88:91], v[40:43]\n\t"
    "ds_read_b64_tr_b16 v[132:133], v158 offset:0\n\t"
    "v_mfma_f32_16x16x32_bf16 v[60:63], v[96:99], v[92:95], v[60:63]\n\t"
    "ds_read_b64_tr_b16 v[134:135], v158 offset:512\n\t"
    "v_mfma_f32_16x16x32_bf16 v[64:67], v[100:103], v[92:95], v[64:67]\n\t"
    "ds_read_b128 v[116:119], v152 offset:24576\n\t"
    "v_mfma_f32_16x16x32_bf16 v[44:47], v[100:103], v[88:91], v[44:47]\n\t"
    "ds_read_b128 v[120:123], v152 offset:25600\n\t"
    "v_mfma_f32_16x16x32_bf16 v[24:27], v[100:103], v[84:87], v[24:27]\n\t"
    "ds_read_b128 v[124:127], v152 offset:26624\n\t"
    "v_mfma_f32_16x16x32_bf16 v[4:7], v[100:103], v[80:83], v[4:7]\n\t"
    "ds_read_b128 v[128:131], v152 offset:27648\n\t"
    "v_mfma_f32_16x16x32_bf16 v[8:11], v[104:107], v[80:83], v[8:11]\n\t"
    "ds_read_b64_tr_b16 v[136:137], v159 offset:0\n\t"
    "v_mfma_f32_16x16x32_bf16 v[28:31], v[104:107], v[84:87], v[28:31]\n\t"
    "ds_read_b64_tr_b16 v[138:139], v159 offset:512\n\t"
    "v_mfma_f32_16x16x32_bf16 v[48:51], v[104:107], v[88:91], v[48:51]\n\t"
    "ds_read_b64_tr_b16 v[140:141], v160 offset:0\n\t"
    "v_mfma_f32_16x16x32_bf16 v[68:71], v[104:107], v[92:95], v[68:71]\n\t"
    "ds_read_b64_tr_b16 v[142:143], v160 offset:512\n\t"
    "v_mfma_f32_16x16x32_bf16 v[72:75], v[108:111], v[92:95], v[72:75]\n\t"
    "ds_read_b64_tr_b16 v[144:145], v161 offset:0\n\t"
    "v_mfma_f32_16x16x32_bf16 v[52:55], v[108:111], v[88:91], v[52:55]\n\t"
    "ds_read_b64_tr_b16 v[146:147], v161 offset:512\n\t"
    "v_mfma_f32_16x16x32_bf16 v[32:35], v[108:111], v[84:87], v[32:35]\n\t"
    "ds_read_b64_tr_b16 v[148:149], v162 offset:0\n\t"
    "v_mfma_f32_16x16x32_bf16 v[12:15], v[108:111], v[80:83], v[12:15]\n\t"
    "ds_read_b64_tr_b16 v[150:151], v162 offset:512\n\t"
    "v_mfma_f32_16x16x32_bf16 v[16:19], v[112:115], v[80:83], v[16:19]\n\t"
    "v_mfma_f32_16x16x32_bf16 v[36:39], v[112:115], v[84:87], v[36:39]\n\t"
    "v_mfma_f32_16x16x32_bf16 v[56:59], v[112:115], v[88:91], v[56:59]\n\t"
    "v_mfma_f32_16x16x32_bf16 v[76:79], v[112:115], v[92:95], v[76:79]\n\t"
    "s_waitcnt lgkmcnt(0)\n\t"
    "v_mfma_f32_16x16x32_bf16 v[0:3], v[132:135], v[116:119], v[0:3]\n\t"
    "v_mfma_f32_16x16x32_bf16 v[20:23], v[132:135], v[120:123], v[20:23]\n\t"
    "s_barrier\n\t"
    "v_mfma_f32_16x16x32_bf16 v[40:43], v[132:135], v[124:127], v[40:43]\n\t"
    "ds_read_b64_tr_b16 v[96:97], v158 offset:20480\n\t"
    "v_mfma_f32_16x16x32_bf16 v[60:63], v[132:135], v[128:131], v[60:63]\n\t"
    "ds_read_b64_tr_b16 v[98:99], v158 offset:20992\n\t"
    "v_mfma_f32_16x16x32_bf16 v[64:67], v[136:139], v[128:131], v[64:67]\n\t"
    "ds_read_b128 v[80:83], v152 offset:32768\n\t"
    "v_mfma_f32_16x16x32_bf16 v[44:47], v[136:139], v[124:127], v[44:47]\n\t"
    "ds_read_b128 v[84:87], v152 offset:33792\n\t"
    "v_mfma_f32_16x16x32_bf16 v[24:27], v[136:139], v[120:123], v[24:27]\n\t"
    "ds_read_b128 v[88:91], v152 offset:34816\n\t"
    "v_mfma_f32_16x16x32_bf16 v[4:7], v[136:139], v[116:119], v[4:7]\n\t"
    "ds_read_b128 v[92:95], v152 offset:35840\n\t"
    "v_mfma_f32_16x16x32_bf16 v[8:11], v[140:143], v[116:119], v[8:11]\n\t"
    "ds_read_b64_tr_b16 v[100:101], v159 offset:20480\n\t"
    "v_mfma_f32_16x16x32_bf16 v[28:31], v[140:143], v[120:123], v[28:31]\n\t"
    "ds_read_b64_tr_b16 v[102:103], v159 offset:20992\n\t"
    "v_mfma_f32_16x16x32_bf16 v[48:51], v[140:143], v[124:127], v[48:51]\n\t"
    "ds_read_b64_tr_b16 v[104:105], v160 offset:20480\n\t"
    "v_mfma_f32_16x16x32_bf16 v[68:71], v[140:143], v[128:131], v[68:71]\n\t"
    "ds_read_b64_tr_b16 v[106:107], v160 offset:20992\n\t"
    "v_mfma_f32_16x16x32_bf16 v[72:75], v[144:147], v[128:131], v[72:75]\n\t"
    "ds_read_b64_tr_b16 v[108:109], v161 offset:20480\n\t"
    "v_mfma_f32_16x16x32_bf16 v[52:55], v[144:147], v[124:127], v[52:55]\n\t"
    "ds_read_b64_tr_b16 v[110:111], v161 offset:20992\n\t"
    "v_mfma_f32_16x16x32_bf16 v[32:35], v[144:147], v[120:123], v[32:35]\n\t"
    "ds_read_b64_tr_b16 v[112:113], v162 offset:20480\n\t"
    "v_mfma_f32_16x16x32_bf16 v[12:15], v[144:147], v[116:119], v[12:15]\n\t"
    "ds_read_b64_tr_b16 v[114:115], v162 offset:20992\n\t"
    "v_mfma_f32_16x16x32_bf16 v[16:19], v[148:151], v[116:119], v[16:19]\n\t"
    "v_mfma_f32_16x16x32_bf16 v[36:39], v[148:151], v[120:123], v[36:39]\n\t"
    "v_mfma_f32_16x16x32_bf16 v[56:59], v[148:151], v[124:127], v[56:59]\n\t"
    "v_mfma_f32_16x16x32_bf16 v[76:79], v[148:151], v[128:131], v[76:79]\n\t"
    "s_waitcnt lgkmcnt(0)\n\t"
    "v_mfma_f32_16x16x32_bf16 v[0:3], v[96:99], v[80:83], v[0:3]\n\t"
    "v_mfma_f32_16x16x32_bf16 v[20:23], v[96:99], v[84:87], v[20:23]\n\t"
    "s_barrier\n\t"
    "v_mfma_f32_16x16x32_bf16 v[40:43], v[96:99], v[88:91], v[40:43]\n\t"
    "ds_read_b64_tr_b16 v[132:133], v153 offset:0\n\t"
    "v_mfma_f32_16x16x32_bf16 v[60:63], v[96:99], v[92:95], v[60:63]\n\t"
    "ds_read_b64_tr_b16 v[134:135], v153 offset:512\n\t"
    "v_mfma_f32_16x16x32_bf16 v[64:67], v[100:103], v[92:95], v[64:67]\n\t"
    "ds_read_b128 v[116:119], v152 offset:0\n\t"
    "v_mfma_f32_16x16x32_bf16 v[44:47], v[100:103], v[88:91], v[44:47]\n\t"
    "ds_read_b128 v[120:123], v152 offset:1024\n\t"
    "v_mfma_f32_16x16x32_bf16 v[24:27], v[100:103], v[84:87], v[24:27]\n\t"
    "ds_read_b128 v[124:127], v152 offset:2048\n\t"
    "v_mfma_f32_16x16x32_bf16 v[4:7], v[100:103], v[80:83], v[4:7]\n\t"
    "ds_read_b128 v[128:131], v152 offset:3072\n\t"
    "v_mfma_f32_16x16x32_bf16 v[8:11], v[104:107], v[80:83], v[8:11]\n\t"
    "ds_read_b64_tr_b16 v[136:137], v154 offset:0\n\t"
    "v_mfma_f32_16x16x32_bf16 v[28:31], v[104:107], v[84:87], v[28:31]\n\t"
    "ds_read_b64_tr_b16 v[138:139], v154 offset:512\n\t"
    "v_mfma_f32_16x16x32_bf16 v[48:51], v[104:107], v[88:91], v[48:51]\n\t"
    "ds_read_b64_tr_b16 v[140:141], v155 offset:0\n\t"
    "v_mfma_f32_16x16x32_bf16 v[68:71], v[104:107], v[92:95], v[68:71]\n\t"
    "ds_read_b64_tr_b16 v[142:143], v155 offset:512\n\t"
    "v_mfma_f32_16x16x32_bf16 v[72:75], v[108:111], v[92:95], v[72:75]\n\t"
    "ds_read_b64_tr_b16 v[144:145], v156 offset:0\n\t"
    "v_mfma_f32_16x16x32_bf16 v[52:55], v[108:111], v[88:91], v[52:55]\n\t"
    "ds_read_b64_tr_b16 v[146:147], v156 offset:512\n\t"
    "v_mfma_f32_16x16x32_bf16 v[32:35], v[108:111], v[84:87], v[32:35]\n\t"
    "ds_read_b64_tr_b16 v[148:149], v157 offset:0\n\t"
    "v_mfma_f32_16x16x32_bf16 v[12:15], v[108:111], v[80:83], v[12:15]\n\t"
    "ds_read_b64_tr_b16 v[150:151], v157 offset:512\n\t"
    "v_mfma_f32_16x16x32_bf16 v[16:19], v[112:115], v[80:83], v[16:19]\n\t"
    "v_mfma_f32_16x16x32_bf16 v[36:39], v[112:115], v[84:87], v[36:39]\n\t"
    "v_mfma_f32_16x16x32_bf16 v[56:59], v[112:115], v[88:91], v[56:59]\n\t"
    "v_mfma_f32_16x16x32_bf16 v[76:79], v[112:115], v[92:95], v[76:79]\n\t"
    "s_waitcnt lgkmcnt(0)\n\t"
    "v_mfma_f32_16x16x32_bf16 v[0:3], v[132:135], v[116:119], v[0:3]\n\t"
    "v_mfma_f32_16x16x32_bf16 v[20:23], v[132:135], v[120:123], v[20:23]\n\t"
    "s_barrier\n\t"
    "v_mfma_f32_16x16x32_bf16 v[40:43], v[132:135], v[124:127], v[40:43]\n\t"
    "ds_read_b64_tr_b16 v[96:97], v153 offset:20480\n\t"
    "v_mfma_f32_16x16x32_bf16 v[60:63], v[132:135], v[128:131], v[60:63]\n\t"
    "ds_read_b64_tr_b16 v[98:99], v153 offset:20992\n\t"
    "v_mfma_f32_16x16x32_bf16 v[64:67], v[136:139], v[128:131], v[64:67]\n\t"
    "ds_read_b128 v[80:83], v152 offset:8192\n\t"
    "v_mfma_f32_16x16x32_bf16 v[44:47], v[136:139], v[124:127], v[44:47]\n\t"
    "ds_read_b128 v[84:87], v152 offset:9216\n\t"
    "v_mfma_f32_16x16x32_bf16 v[24:27], v[136:139], v[120:123], v[24:27]\n\t"
    "ds_read_b128 v[88:91], v152 offset:10240\n\t"
    "v_mfma_f32_16x16x32_bf16 v[4:7], v[136:139], v[116:119], v[4:7]\n\t"
    "ds_read_b128 v[92:95], v152 offset:11264\n\t"
    "v_mfma_f32_16x16x32_bf16 v[8:11], v[140:143], v[116:119], v[8:11]\n\t"
    "ds_read_b64_tr_b16 v[100:101], v154 offset:20480\n\t"
    "v_mfma_f32_16x16x32_bf16 v[28:31], v[140:143], v[120:123], v[28:31]\n\t"
    "ds_read_b64_tr_b16 v[102:103], v154 offset:20992\n\t"
    "v_mfma_f32_16x16x32_bf16 v[48:51], v[140:143], v[124:127], v[48:51]\n\t"
    "ds_read_b64_tr_b16 v[104:105], v155 offset:20480\n\t"
    "v_mfma_f32_16x16x32_bf16 v[68:71], v[140:143], v[128:131], v[68:71]\n\t"
    "ds_read_b64_tr_b16 v[106:107], v155 offset:20992\n\t"
    "v_mfma_f32_16x16x32_bf16 v[72:75], v[144:147], v[128:131], v[72:75]\n\t"
    "ds_read_b64_tr_b16 v[108:109], v156 offset:20480\n\t"
    "v_mfma_f32_16x16x32_bf16 v[52:55], v[144:147], v[124:127], v[52:55]\n\t"
    "ds_read_b64_tr_b16 v[110:111], v156 offset:20992\n\t"
    "v_mfma_f32_16x16x32_bf16 v[32:35], v[144:147], v[120:123], v[32:35]\n\t"
    "ds_read_b64_tr_b16 v[112:113], v157 offset:20480\n\t"
    "v_mfma_f32_16x16x32_bf16 v[12:15], v[144:147], v[116:119], v[12:15]\n\t"
    "ds_read_b64_tr_b16 v[114:115], v157 offset:20992\n\t"
    "v_mfma_f32_16x16x32_bf16 v[16:19], v[148:151], v[116:119], v[16:19]\n\t"
    "v_mfma_f32_16x16x32_bf16 v[36:39], v[148:151], v[120:123], v[36:39]\n\t"
    "v_mfma_f32_16x16x32_bf16 v[56:59], v[148:151], v[124:127], v[56:59]\n\t"
    "v_mfma_f32_16x16x32_bf16 v[76:79], v[148:151], v[128:131], v[76:79]\n\t"
    "s_waitcnt lgkmcnt(0)\n\t"
    "v_mfma_f32_16x16x32_bf16 v[0:3], v[96:99], v[80:83], v[0:3]\n\t"
    "v_mfma_f32_16x16x32_bf16 v[20:23], v[96:99], v[84:87], v[20:23]\n\t"
    "s_barrier\n\t"
    "s_setprio 1\n\t"
    "s_cmp_gt_u32 %[wv], 3\n\t"
    "s_cbranch_scc1 Lpfp_%=\n\t"
    "s_setprio 2\n\t"
    "Lpfp_%=:\n\t"
    "v_mfma_f32_16x16x32_bf16 v[40:43], v[96:99], v[88:91], v[40:43]\n\t"
    "ds_read_b64_tr_b16 v[132:133], v153 offset:40960\n\t"
    "v_mfma_f32_16x16x32_bf16 v[60:63], v[96:99], v[92:95], v[60:63]\n\t"
    "ds_read_b64_tr_b16 v[134:135], v153 offset:41472\n\t"
    "v_mfma_f32_16x16x32_bf16 v[64:67], v[100:103], v[92:95], v[64:67]\n\t"
    "ds_read_b128 v[116:119], v152 offset:16384\n\t"
    "v_mfma_f32_16x16x32_bf16 v[44:47], v[100:103], v[88:91], v[44:47]\n\t"
    "ds_read_b128 v[120:123], v152 offset:17408\n\t"
    "v_mfma_f32_16x16x32_bf16 v[24:27], v[100:103], v[84:87], v[24:27]\n\t"
    "ds_read_b128 v[124:127], v152 offset:18432\n\t"
    "v_mfma_f32_16x16x32_bf16 v[4:7], v[100:103], v[80:83], v[4:7]\n\t"
    "ds_read_b128 v[128:131], v152 offset:19456\n\t"
    "v_mfma_f32_16x16x32_bf16 v[8:11], v[104:107], v[80:83], v[8:11]\n\t"
    "ds_read_b64_tr_b16 v[136:137], v154 offset:40960\n\t"
    "v_mfma_f32_16x16x32_bf16 v[28:31], v[104:107], v[84:87], v[28:31]\n\t"
    "ds_read_b64_tr_b16 v[138:139], v154 offset:41472\n\t"
    "v_mfma_f32_16x16x32_bf16 v[48:51], v[104:107], v[88:91], v[48:51]\n\t"
    "ds_read_b64_tr_b16 v[140:141], v155 offset:40960\n\t"
    "v_mfma_f32_16x16x32_bf16 v[68:71], v[104:107], v[92:95], v[68:71]\n\t"
    "ds_read_b64_tr_b16 v[142:143], v155 offset:41472\n\t"
    "v_mfma_f32_16x16x32_bf16 v[72:75], v[108:111], v[92:95], v[72:75]\n\t"
    "ds_read_b64_tr_b16 v[144:145], v156 offset:40960\n\t"
    "v_mfma_f32_16x16x32_bf16 v[52:55], v[108:111], v[88:91], v[52:55]\n\t"
    "ds_read_b64_tr_b16 v[146:147], v156 offset:41472\n\t"
    "v_mfma_f32_16x16x32_bf16 v[32:35], v[108:111], v[84:87], v[32:35]\n\t"
    "ds_read_b64_tr_b16 v[148:149], v157 offset:40960\n\t"
    "v_mfma_f32_16x16x32_bf16 v[12:15], v[108:111], v[80:83], v[12:15]\n\t"
    "ds_read_b64_tr_b16 v[150:151], v157 offset:41472\n\t"
    "v_mfma_f32_16x16x32_bf16 v[16:19], v[112:115], v[80:83], v[16:19]\n\t"
    "v_mfma_f32_16x16x32_bf16 v[36:39], v[112:115], v[84:87], v[36:39]\n\t"
    "v_mfma_f32_16x16x32_bf16 v[56:59], v[112:115], v[88:91], v[56:59]\n\t"
    "v_mfma_f32_16x16x32_bf16 v[76:79], v[112:115], v[92:95], v[76:79]\n\t"
    "s_waitcnt lgkmcnt(0)\n\t"
    "v_mfma_f32_16x16x32_bf16 v[0:3], v[132:135], v[116:119], v[0:3]\n\t"
    "v_mfma_f32_16x16x32_bf16 v[20:23], v[132:135], v[120:123], v[20:23]\n\t"
    "v_mfma_f32_16x16x32_bf16 v[40:43], v[132:135], v[124:127], v[40:43]\n\t"
    "ds_read_b64_tr_b16 v[96:97], v158 offset:0\n\t"
    "v_mfma_f32_16x16x32_bf16 v[60:63], v[132:135], v[128:131], v[60:63]\n\t"
    "ds_read_b64_tr_b16 v[98:99], v158 offset:512\n\t"
    "v_mfma_f32_16x16x32_bf16 v[64:67], v[136:139], v[128:131], v[64:67]\n\t"
    "ds_read_b128 v[80:83], v152 offset:24576\n\t"
    "v_mfma_f32_16x16x32_bf16 v[44:47], v[136:139], v[124:127], v[44:47]\n\t"
    "ds_read_b128 v[84:87], v152 offset:25600\n\t"
    "v_mfma_f32_16x16x32_bf16 v[24:27], v[136:139], v[120:123], v[24:27]\n\t"
    "ds_read_b128 v[88:91], v152 offset:26624\n\t"
    "v_mfma_f32_16x16x32_bf16 v[4:7], v[136:139], v[116:119], v[4:7]\n\t"
    "ds_read_b128 v[92:95], v152 offset:27648\n\t"
    "v_mfma_f32_16x16x32_bf16 v[8:11], v[140:143], v[116:119], v[8:11]\n\t"
    "ds_read_b64_tr_b16 v[100:101], v159 offset:0\n\t"
    "v_mfma_f32_16x16x32_bf16 v[28:31], v[140:143], v[120:123], v[28:31]\n\t"
    "ds_read_b64_tr_b16 v[102:103], v159 offset:512\n\t"
    "v_mfma_f32_16x16x32_bf16 v[48:51], v[140:143], v[124:127], v[48:51]\n\t"
    "ds_read_b64_tr_b16 v[104:105], v160 offset:0\n\t"
    "v_mfma_f32_16x16x32_bf16 v[68:71], v[140:143], v[128:131], v[68:71]\n\t"
    "ds_read_b64_tr_b16 v[106:107], v160 offset:512\n\t"
    "v_mfma_f32_16x16x32_bf16 v[72:75], v[144:147], v[128:131], v[72:75]\n\t"
    "ds_read_b64_tr_b16 v[108:109], v161 offset:0\n\t"
    "v_mfma_f32_16x16x32_bf16 v[52:55], v[144:147], v[124:127], v[52:55]\n\t"
    "ds_read_b64_tr_b16 v[110:111], v161 offset:512\n\t"
    "v_mfma_f32_16x16x32_bf16 v[32:35], v[144:147], v[120:123], v[32:35]\n\t"
    "ds_read_b64_tr_b16 v[112:113], v162 offset:0\n\t"
    "v_mfma_f32_16x16x32_bf16 v[12:15], v[144:147], v[116:119], v[12:15]\n\t"
    "ds_read_b64_tr_b16 v[114:115], v162 offset:512\n\t"
    "v_mfma_f32_16x16x32_bf16 v[16:19], v[148:151], v[116:119], v[16:19]\n\t"
    "v_mfma_f32_16x16x32_bf16 v[36:39], v[148:151], v[120:123], v[36:39]\n\t"
    "v_mfma_f32_16x16x32_bf16 v[56:59], v[148:151], v[124:127], v[56:59]\n\t"
    "v_mfma_f32_16x16x32_bf16 v[76:79], v[148:151], v[128:131], v[76:79]\n\t"
    "s_waitcnt lgkmcnt(0)\n\t"
    "v_mfma_f32_16x16x32_bf16 v[0:3], v[96:99], v[80:83], v[0:3]\n\t"
    "v_mfma_f32_16x16x32_bf16 v[20:23], v[96:99], v[84:87], v[20:23]\n\t"
    "v_mfma_f32_16x16x32_bf16 v[40:43], v[96:99], v[88:91], v[40:43]\n\t"
    "ds_read_b64_tr_b16 v[132:133], v158 offset:20480\n\t"
    "v_mfma_f32_16x16x32_bf16 v[60:63], v[96:99], v[92:95], v[60:63]\n\t"
    "ds_read_b64_tr_b16 v[134:135], v158 offset:20992\n\t"
    "v_mfma_f32_16x16x32_bf16 v[64:67], v[100:103], v[92:95], v[64:67]\n\t"
    "ds_read_b128 v[116:119], v152 offset:32768\n\t"
    "v_mfma_f32_16x16x32_bf16 v[44:47], v[100:103], v[88:91], v[44:47]\n\t"
    "ds_read_b128 v[120:123], v152 offset:33792\n\t"
    "v_mfma_f32_16x16x32_bf16 v[24:27], v[100:103], v[84:87], v[24:27]\n\t"
    "ds_read_b128 v[124:127], v152 offset:34816\n\t"
    "v_mfma_f32_16x16x32_bf16 v[4:7], v[100:103], v[80:83], v[4:7]\n\t"
    "ds_read_b128 v[128:131], v152 offset:35840\n\t"
    "v_mfma_f32_16x16x32_bf16 v[8:11], v[104:107], v[80:83], v[8:11]\n\t"
    "ds_read_b64_tr_b16 v[136:137], v159 offset:20480\n\t"
    "v_mfma_f32_16x16x32_bf16 v[28:31], v[104:107], v[84:87], v[28:31]\n\t"
    "ds_read_b64_tr_b16 v[138:139], v159 offset:20992\n\t"
    "v_mfma_f32_16x16x32_bf16 v[48:51], v[104:107], v[88:91], v[48:51]\n\t"
    "ds_read_b64_tr_b16 v[140:141], v160 offset:20480\n\t"
    "v_mfma_f32_16x16x32_bf16 v[68:71], v[104:107], v[92:95], v[68:71]\n\t"
    "ds_read_b64_tr_b16 v[142:143], v160 offset:20992\n\t"
    "v_mfma_f32_16x16x32_bf16 v[72:75], v[108:111], v[92:95], v[72:75]\n\t"
    "ds_read_b64_tr_b16 v[144:145], v161 offset:20480\n\t"
    "v_mfma_f32_16x16x32_bf16 v[52:55], v[108:111], v[88:91], v[52:55]\n\t"
    "ds_read_b64_tr_b16 v[146:147], v161 offset:20992\n\t"
    "v_mfma_f32_16x16x32_bf16 v[32:35], v[108:111], v[84:87], v[32:35]\n\t"
    "ds_read_b64_tr_b16 v[148:149], v162 offset:20480\n\t"
    "v_mfma_f32_16x16x32_bf16 v[12:15], v[108:111], v[80:83], v[12:15]\n\t"
    "ds_read_b64_tr_b16 v[150:151], v162 offset:20992\n\t"
    "v_mfma_f32_16x16x32_bf16 v[16:19], v[112:115], v[80:83], v[16:19]\n\t"
    "v_mfma_f32_16x16x32_bf16 v[36:39], v[112:115], v[84:87], v[36:39]\n\t"
    "v_mfma_f32_16x16x32_bf16 v[56:59], v[112:115], v[88:91], v[56:59]\n\t"
    "v_mfma_f32_16x16x32_bf16 v[76:79], v[112:115], v[92:95], v[76:79]\n\t"
    "s_waitcnt lgkmcnt(0)\n\t"
    "s_mov_b64 exec, 1\n\t"
    "ds_add_u32 v164, v165\n\t"
    "s_mov_b64 exec, -1\n\t"
    "s_cmp_eq_u32 %[xa], 0\n\t"
    "s_cbranch_scc1 Lq_%=\n\t"
    "s_cmp_eq_u32 %[xa], 1\n\t"
    "s_cbranch_scc1 Lk_%=\n\t"
    "s_cmp_eq_u32 %[ob], 0\n\t"
    "s_cbranch_scc1 Lend_%=\n\t"
    "v_mfma_f32_16x16x32_bf16 v[0:3], v[132:135], v[116:119], v[0:3]\n\t"
    "v_mfma_f32_16x16x32_bf16 v[4:7], v[136:139], v[116:119], v[4:7]\n\t"
    "v_mfma_f32_16x16x32_bf16 v[8:11], v[140:143], v[116:119], v[8:11]\n\t"
    "v_mfma_f32_16x16x32_bf16 v[12:15], v[144:147], v[116:119], v[12:15]\n\t"
    "v_mfma_f32_16x16x32_bf16 v[16:19], v[148:151], v[116:119], v[16:19]\n\t"
    "v_mfma_f32_16x16x32_bf16 v[20:23], v[132:135], v[120:123], v[20:23]\n\t"
    "v_mfma_f32_16x16x32_bf16 v[24:27], v[136:139], v[120:123], v[24:27]\n\t"
    "v_mfma_f32_16x16x32_bf16 v[28:31], v[140:143], v[120:123], v[28:31]\n\t"
    "v_mfma_f32_16x16x32_bf16 v[32:35], v[144:147], v[120:123], v[32:35]\n\t"
    "v_mfma_f32_16x16x32_bf16 v[36:39], v[148:151], v[120:123], v[36:39]\n\t"
    "v_mfma_f32_16x16x32_bf16 v[40:43], v[132:135], v[124:127], v[40:43]\n\t"
    "v_permlane32_swap_b32 v0, v4\n\t"
    "v_permlane32_swap_b32 v1, v5\n\t"
    "v_permlane32_swap_b32 v2, v6\n\t"
    "v_permlane32_swap_b32 v3, v7\n\t"
    "v_permlane32_swap_b32 v8, v12\n\t"
    "v_permlane32_swap_b32 v9, v13\n\t"
    "v_permlane32_swap_b32 v10, v14\n\t"
    "v_permlane32_swap_b32 v11, v15\n\t"
    "v_pk_mul_f32 v[80:81], v[0:1], %[nl]\n\t"
    "v_mfma_f32_16x16x32_bf16 v[44:47], v[136:139], v[124:127], v[44:47]\n\t"
    "v_pk_mul_f32 v[82:83], v[2:3], %[nl]\n\t"
    "v_pk_mul_f32 v[84:85], v[8:9], %[nl]\n\t"
    "v_pk_mul_f32 v[86:87], v[10:11], %[nl]\n\t"
    "v_exp_f32 v80, v80\n\t"
    "v_exp_f32 v81, v81\n\t"
    "v_exp_f32 v82, v82\n\t"
    "v_exp_f32 v83, v83\n\t"
    "v_exp_f32 v84, v84\n\t"
    "v_exp_f32 v85, v85\n\t"
    "v_mfma_f32_16x16x32_bf16 v[48:51], v[140:143], v[124:127], v[48:51]\n\t"
    "v_exp_f32 v86, v86\n\t"
    "v_exp_f32 v87, v87\n\t"
    "v_pk_mul_f32 v[0:1], v[0:1], v[4:5]\n\t"
    "v_pk_mul_f32 v[2:3], v[2:3], v[6:7]\n\t"
    "v_pk_mul_f32 v[8:9], v[8:9], v[12:13]\n\t"
    "v_pk_mul_f32 v[10:11], v[10:11], v[14:15]\n\t"
    "v_pk_add_f32 v[80:81], v[80:81], 1.0 op_sel_hi:[1,0]\n\t"
    "v_pk_add_f32 v[82:83], v[82:83], 1.0 op_sel_hi:[1,0]\n\t"
    "v_pk_add_f32 v[84:85], v[84:85], 1.0 op_sel_hi:[1,0]\n\t"
    "v_mfma_f32_16x16x32_bf16 v[52:55], v[144:147], v[124:127], v[52:55]\n\t"
    "v_pk_add_f32 v[86:87], v[86:87], 1.0 op_sel_hi:[1,0]\n\t"
    "v_rcp_f32 v80, v80\n\t"
    "v_rcp_f32 v81, v81\n\t"
    "v_rcp_f32 v82, v82\n\t"
    "v_rcp_f32 v83, v83\n\t"
    "v_rcp_f32 v84, v84\n\t"
    "v_rcp_f32 v85, v85\n\t"
    "v_rcp_f32 v86, v86\n\t"
    "v_rcp_f32 v87, v87\n\t"
    "v_mfma_f32_16x16x32_bf16 v[56:59], v[148:151], v[124:127], v[56:59]\n\t"
    "s_nop 0\n\t"
    "v_pk_mul_f32 v[0:1], v[0:1], v[80:81]\n\t"
    "v_pk_mul_f32 v[2:3], v[2:3], v[82:83]\n\t"
    "v_pk_mul_f32 v[8:9], v[8:9], v[84:85]\n\t"
    "v_pk_mul_f32 v[10:11], v[10:11], v[86:87]\n\t"
    "v_cvt_pk_bf16_f32 v156, v0, v1\n\t"
    "v_cvt_pk_bf16_f32 v157, v2, v3\n\t"
    "v_cvt_pk_bf16_f32 v158, v8, v9\n\t"
    "v_cvt_pk_bf16_f32 v159, v10, v11\n\t"
    "v_mfma_f32_16x16x32_bf16 v[60:63], v[132:135], v[128:131], v[60:63]\n\t"
    "v_permlane32_swap_b32 v20, v24\n\t"
    "v_permlane32_swap_b32 v21, v25\n\t"
    "v_permlane32_swap_b32 v22, v26\n\t"
    "v_permlane32_swap_b32 v23, v27\n\t"
    "v_permlane32_swap_b32 v28, v32\n\t"
    "v_permlane32_swap_b32 v29, v33\n\t"
    "v_permlane32_swap_b32 v30, v34\n\t"
    "v_permlane32_swap_b32 v31, v35\n\t"
    "v_pk_mul_f32 v[80:81], v[20:21], %[nl]\n\t"
    "v_mfma_f32_16x16x32_bf16 v[64:67], v[136:139], v[128:131], v[64:67]\n\t"
    "v_pk_mul_f32 v[82:83], v[22:23], %[nl]\n\t"
    "v_pk_mul_f32 v[84:85], v[28:29], %[nl]\n\t"
    "v_pk_mul_f32 v[86:87], v[30:31], %[nl]\n\t"
    "v_exp_f32 v80, v80\n\t"
    "v_exp_f32 v81, v81\n\t"
    "v_exp_f32 v82, v82\n\t"
    "v_exp_f32 v83, v83\n\t"
    "v_exp_f32 v84, v84\n\t"
    "v_exp_f32 v85, v85\n\t"
    "v_mfma_f32_16x16x32_bf16 v[68:71], v[140:143], v[128:131], v[68:71]\n\t"
    "v_exp_f32 v86, v86\n\t"
    "v_exp_f32 v87, v87\n\t"
    "v_pk_mul_f32 v[20:21], v[20:21], v[24:25]\n\t"
    "v_pk_mul_f32 v[22:23], v[22:23], v[26:27]\n\t"
    "v_pk_mul_f32 v[28:29], v[28:29], v[32:33]\n\t"
    "v_pk_mul_f32 v[30:31], v[30:31], v[34:35]\n\t"
    "v_pk_add_f32 v[80:81], v[80:81], 1.0 op_sel_hi:[1,0]\n\t"
    "v_pk_add_f32 v[82:83], v[82:83], 1.0 op_sel_hi:[1,0]\n\t"
    "v_pk_add_f32 v[84:85], v[84:85], 1.0 op_sel_hi:[1,0]\n\t"
    "v_mfma_f32_16x16x32_bf16 v[72:75], v[144:147], v[128:131], v[72:75]\n\t"
    "v_pk_add_f32 v[86:87], v[86:87], 1.0 op_sel_hi:[1,0]\n\t"
    "v_rcp_f32 v80, v80\n\t"
    "v_rcp_f32 v81, v81\n\t"
    "v_rcp_f32 v82, v82\n\t"
    "v_rcp_f32 v83, v83\n\t"
    "v_rcp_f32 v84, v84\n\t"
    "v_rcp_f32 v85, v85\n\t"
    "v_rcp_f32 v86, v86\n\t"
    "v_rcp_f32 v87, v87\n\t"
    "v_mfma_f32_16x16x32_bf16 v[76:79], v[148:151], v[128:131], v[76:79]\n\t"
    "s_nop 0\n\t"
    "v_pk_mul_f32 v[20:21], v[20:21], v[80:81]\n\t"
    "v_pk_mul_f32 v[22:23], v[22:23], v[82:83]\n\t"
    "v_pk_mul_f32 v[28:29], v[28:29], v[84:85]\n\t"
    "v_pk_mul_f32 v[30:31], v[30:31], v[86:87]\n\t"
    "v_cvt_pk_bf16_f32 v160, v20, v21\n\t"
    "v_cvt_pk_bf16_f32 v161, v22, v23\n\t"
    "v_cvt_pk_bf16_f32 v162, v28, v29\n\t"
    "v_cvt_pk_bf16_f32 v163, v30, v31\n\t"
    "s_setprio 0\n\t"
    "v_permlane32_swap_b32 v40, v44\n\t"
    "v_permlane32_swap_b32 v41, v45\n\t"
    "v_permlane32_swap_b32 v42, v46\n\t"
    "v_permlane32_swap_b32 v43, v47\n\t"
    "v_permlane32_swap_b32 v48, v52\n\t"
    "v_permlane32_swap_b32 v49, v53\n\t"
    "v_permlane32_swap_b32 v50, v54\n\t"
    "v_permlane32_swap_b32 v51, v55\n\t"
    "v_pk_mul_f32 v[80:81], v[40:41], %[nl]\n\t"
    "v_pk_mul_f32 v[82:83], v[42:43], %[nl]\n\t"
    "v_pk_mul_f32 v[84:85], v[48:49], %[nl]\n\t"
    "v_pk_mul_f32 v[86:87], v[50:51], %[nl]\n\t"
    "v_exp_f32 v80, v80\n\t"
    "v_exp_f32 v81, v81\n\t"
    "v_exp_f32 v82, v82\n\t"
    "v_exp_f32 v83, v83\n\t"
    "v_exp_f32 v84, v84\n\t"
    "v_exp_f32 v85, v85\n\t"
    "v_exp_f32 v86, v86\n\t"
    "v_exp_f32 v87, v87\n\t"
    "v_pk_mul_f32 v[40:41], v[40:41], v[44:45]\n\t"
    "v_pk_mul_f32 v[42:43], v[42:43], v[46:47]\n\t"
    "v_pk_mul_f32 v[48:49], v[48:49], v[52:53]\n\t"
    "v_pk_mul_f32 v[50:51], v[50:51], v[54:55]\n\t"
    "v_pk_add_f32 v[80:81], v[80:81], 1.0 op_sel_hi:[1,0]\n\t"
    "v_pk_add_f32 v[82:83], v[82:83], 1.0 op_sel_hi:[1,0]\n\t"
    "v_pk_add_f32 v[84:85], v[84:85], 1.0 op_sel_hi:[1,0]\n\t"
    "v_pk_add_f32 v[86:87], v[86:87], 1.0 op_sel_hi:[1,0]\n\t"
    "v_rcp_f32 v80, v80\n\t"
    "v_rcp_f32 v81, v81\n\t"
    "v_rcp_f32 v82, v82\n\t"
    "v_rcp_f32 v83, v83\n\t"
    "v_rcp_f32 v84, v84\n\t"
    "v_rcp_f32 v85, v85\n\t"
    "v_rcp_f32 v86, v86\n\t"
    "v_rcp_f32 v87, v87\n\t"
    "s_nop 0\n\t"
    "v_pk_mul_f32 v[40:41], v[40:41], v[80:81]\n\t"
    "v_pk_mul_f32 v[42:43], v[42:43], v[82:83]\n\t"
    "v_pk_mul_f32 v[48:49], v[48:49], v[84:85]\n\t"
    "v_pk_mul_f32 v[50:51], v[50:51], v[86:87]\n\t"
    "v_cvt_pk_bf16_f32 v144, v40, v41\n\t"
    "v_cvt_pk_bf16_f32 v145, v42, v43\n\t"
    "v_cvt_pk_bf16_f32 v146, v48, v49\n\t"
    "v_cvt_pk_bf16_f32 v147, v50, v51\n\t"
    "v_permlane32_swap_b32 v60, v64\n\t"
    "v_permlane32_swap_b32 v61, v65\n\t"
    "v_permlane32_swap_b32 v62, v66\n\t"
    "v_permlane32_swap_b32 v63, v67\n\t"
    "v_permlane32_swap_b32 v68, v72\n\t"
    "v_permlane32_swap_b32 v69, v73\n\t"
    "v_permlane32_swap_b32 v70, v74\n\t"
    "v_permlane32_swap_b32 v71, v75\n\t"
    "v_pk_mul_f32 v[80:81], v[60:61], %[nl]\n\t"
    "v_pk_mul_f32 v[82:83], v[62:63], %[nl]\n\t"
    "v_pk_mul_f32 v[84:85], v[68:69], %[nl]\n\t"
    "v_pk_mul_f32 v[86:87], v[70:71], %[nl]\n\t"
    "v_exp_f32 v80, v80\n\t"
    "v_exp_f32 v81, v81\n\t"
    "v_exp_f32 v82, v82\n\t"
    "v_exp_f32 v83, v83\n\t"
    "v_exp_f32 v84, v84\n\t"
    "v_exp_f32 v85, v85\n\t"
    "v_exp_f32 v86, v86\n\t"
    "v_exp_f32 v87, v87\n\t"
    "v_pk_mul_f32 v[60:61], v[60:61], v[64:65]\n\t"
    "v_pk_mul_f32 v[62:63], v[62:63], v[66:67]\n\t"
    "v_pk_mul_f32 v[68:69], v[68:69], v[72:73]\n\t"
    "v_pk_mul_f32 v[70:71], v[70:71], v[74:75]\n\t"
    "v_pk_add_f32 v[80:81], v[80:81], 1.0 op_sel_hi:[1,0]\n\t"
    "v_pk_add_f32 v[82:83], v[82:83], 1.0 op_sel_hi:[1,0]\n\t"
    "v_pk_add_f32 v[84:85], v[84:85], 1.0 op_sel_hi:[1,0]\n\t"
    "v_pk_add_f32 v[86:87], v[86:87], 1.0 op_sel_hi:[1,0]\n\t"
    "v_rcp_f32 v80, v80\n\t"
    "v_rcp_f32 v81, v81\n\t"
    "v_rcp_f32 v82, v82\n\t"
    "v_rcp_f32 v83, v83\n\t"
    "v_rcp_f32 v84, v84\n\t"
    "v_rcp_f32 v85, v85\n\t"
    "v_rcp_f32 v86, v86\n\t"
    "v_rcp_f32 v87, v87\n\t"
    "s_nop 0\n\t"
    "v_pk_mul_f32 v[60:61], v[60:61], v[80:81]\n\t"
    "v_pk_mul_f32 v[62:63], v[62:63], v[82:83]\n\t"
    "v_pk_mul_f32 v[68:69], v[68:69], v[84:85]\n\t"
    "v_pk_mul_f32 v[70:71], v[70:71], v[86:87]\n\t"
    "v_cvt_pk_bf16_f32 v148, v60, v61\n\t"
    "v_cvt_pk_bf16_f32 v149, v62, v63\n\t"
    "v_cvt_pk_bf16_f32 v150, v68, v69\n\t"
    "v_cvt_pk_bf16_f32 v151, v70, v71\n\t"
    "v_permlane32_swap_b32 v16, v36\n\t"
    "v_permlane32_swap_b32 v17, v37\n\t"
    "v_permlane32_swap_b32 v18, v38\n\t"
    "v_permlane32_swap_b32 v19, v39\n\t"
    "v_permlane32_swap_b32 v56, v76\n\t"
    "v_permlane32_swap_b32 v57, v77\n\t"
    "v_permlane32_swap_b32 v58, v78\n\t"
    "v_permlane32_swap_b32 v59, v79\n\t"
    "v_pk_mul_f32 v[80:81], v[16:17], %[nl]\n\t"
    "v_pk_mul_f32 v[82:83], v[18:19], %[nl]\n\t"
    "v_pk_mul_f32 v[84:85], v[56:57], %[nl]\n\t"
    "v_pk_mul_f32 v[86:87], v[58:59], %[nl]\n\t"
    "v_exp_f32 v80, v80\n\t"
    "v_exp_f32 v81, v81\n\t"
    "v_exp_f32 v82, v82\n\t"
    "v_exp_f32 v83, v83\n\t"
    "v_exp_f32 v84, v84\n\t"
    "v_exp_f32 v85, v85\n\t"
    "v_exp_f32 v86, v86\n\t"
    "v_exp_f32 v87, v87\n\t"
    "v_pk_mul_f32 v[16:17], v[16:17], v[36:37]\n\t"
    "v_pk_mul_f32 v[18:19], v[18:19], v[38:39]\n\t"
    "v_pk_mul_f32 v[56:57], v[56:57], v[76:77]\n\t"
    "v_pk_mul_f32 v[58:59], v[58:59], v[78:79]\n\t"
    "v_pk_add_f32 v[80:81], v[80:81], 1.0 op_sel_hi:[1,0]\n\t"
    "v_pk_add_f32 v[82:83], v[82:83], 1.0 op_sel_hi:[1,0]\n\t"
    "v_pk_add_f32 v[84:85], v[84:85], 1.0 op_sel_hi:[1,0]\n\t"
    "v_pk_add_f32 v[86:87], v[86:87], 1.0 op_sel_hi:[1,0]\n\t"
    "v_rcp_f32 v80, v80\n\t"
    "v_rcp_f32 v81, v81\n\t"
    "v_rcp_f32 v82, v82\n\t"
    "v_rcp_f32 v83, v83\n\t"
    "v_rcp_f32 v84, v84\n\t"
    "v_rcp_f32 v85, v85\n\t"
    "v_rcp_f32 v86, v86\n\t"
    "v_rcp_f32 v87, v87\n\t"
    "s_nop 0\n\t"
    "v_pk_mul_f32 v[16:17], v[16:17], v[80:81]\n\t"
    "v_pk_mul_f32 v[18:19], v[18:19], v[82:83]\n\t"
    "v_pk_mul_f32 v[56:57], v[56:57], v[84:85]\n\t"
    "v_pk_mul_f32 v[58:59], v[58:59], v[86:87]\n\t"
    "v_cvt_pk_bf16_f32 v152, v16, v17\n\t"
    "v_cvt_pk_bf16_f32 v153, v18, v19\n\t"
    "v_cvt_pk_bf16_f32 v154, v56, v57\n\t"
    "v_cvt_pk_bf16_f32 v155, v58, v59\n\t"
    "v_mbcnt_lo_u32_b32 v0, -1, 0\n\t"
    "v_mbcnt_hi_u32_b32 v0, -1, v0\n\t"
    "v_and_b32 v1, 15, v0\n\t"
    "v_lshlrev_b32 v1, 4, v1\n\t"
    "v_bfe_u32 v2, v0, 4, 1\n\t"
    "v_lshl_add_u32 v1, v2, 3, v1\n\t"
    "v_lshrrev_b32 v2, 5, v0\n\t"
    "v_mul_u32_u24 v2, 0xc000, v2\n\t"
    "v_add_u32 v1, v1, v2\n\t"
    "v_lshlrev_b32 v0, 4, v0\n\t"
    "buffer_store_dwordx4 v[156:159], v0, %[rs], %[ob] offen offset:0 sc1\n\t"
    "s_add_u32 %[so], %[ob], 49152\n\t"
    "buffer_store_dwordx4 v[160:163], v0, %[rs], %[so] offen offset:0 sc1\n\t"
    "s_add_u32 %[so], %[ob], 98304\n\t"
    "buffer_store_dwordx4 v[144:147], v0, %[rs], %[so] offen offset:0 sc1\n\t"
    "s_add_u32 %[so], %[ob], 147456\n\t"
    "buffer_store_dwordx4 v[148:151], v0, %[rs], %[so] offen offset:0 sc1\n\t"
    "s_cmp_eq_u32 %[o2], 0\n\t"
    "s_cbranch_scc1 Lend_%=\n\t"
    "s_add_u32 %[so], %[o2], 0x18000\n\t"
    "buffer_store_dwordx2 v[152:153], v1, %[rs], %[o2] offen sc1\n\t"
    "buffer_store_dwordx2 v[154:155], v1, %[rs], %[so] offen sc1\n\t"
    "s_branch Lend_%=\n\t"
    "Lq_%=:\n\t"
    "v_mfma_f32_16x16x32_bf16 v[0:3], v[132:135], v[116:119], v[0:3]\n\t"
    "v_mbcnt_lo_u32_b32 v152, -1, 0\n\t"
    "v_mbcnt_hi_u32_b32 v152, -1, v152\n\t"
    "v_and_b32 v153, 15, v152\n\t"
    "v_lshrrev_b32 v154, 4, v152\n\t"
    "v_mfma_f32_16x16x32_bf16 v[20:23], v[132:135], v[120:123], v[20:23]\n\t"
    "v_and_b32 v155, 1, v154\n\t"
    "v_lshlrev_b32 v155, 9, v155\n\t"
    "v_lshrrev_b32 v157, 5, v152\n\t"
    "v_mfma_f32_16x16x32_bf16 v[40:43], v[132:135], v[124:127], v[40:43]\n\t"
    "v_lshlrev_b32 v156, 4, v153\n\t"
    "v_lshl_add_u32 v157, v157, 8, v156\n\t"
    "v_add_u32 v155, v155, v157\n\t"
    "v_mfma_f32_16x16x32_bf16 v[60:63], v[132:135], v[128:131], v[60:63]\n\t"
    "v_mul_u32_u24 v154, 81920, v154\n\t"
    "v_add_u32 v156, v156, v154\n\t"
    "s_mov_b32 %[so], %[ob]\n\t"
    "v_mfma_f32_16x16x32_bf16 v[4:7], v[136:139], v[116:119], v[4:7]\n\t"
    "v_mfma_f32_16x16x32_bf16 v[24:27], v[136:139], v[120:123], v[24:27]\n\t"
    "v_mfma_f32_16x16x32_bf16 v[44:47], v[136:139], v[124:127], v[44:47]\n\t"
    "v_mfma_f32_16x16x32_bf16 v[64:67], v[136:139], v[128:131], v[64:67]\n\t"
    "v_mfma_f32_16x16x32_bf16 v[8:11], v[140:143], v[116:119], v[8:11]\n\t"
    "v_pk_mul_f32 v[0:1], v[0:1], %[sc]\n\t"
    "v_pk_mul_f32 v[2:3], v[2:3], %[sc]\n\t"
    "v_pk_mul_f32 v[20:21], v[20:21], %[sc]\n\t"
    "v_pk_mul_f32 v[22:23], v[22:23], %[sc]\n\t"
    "v_pk_mul_f32 v[40:41], v[40:41], %[sc]\n\t"
    "v_pk_mul_f32 v[42:43], v[42:43], %[sc]\n\t"
    "v_mfma_f32_16x16x32_bf16 v[28:31], v[140:143], v[120:123], v[28:31]\n\t"
    "v_pk_mul_f32 v[60:61], v[60:61], %[sc]\n\t"
    "v_pk_mul_f32 v[62:63], v[62:63], %[sc]\n\t"
    "v_cvt_pk_bf16_f32 v80, v0, v1\n\t"
    "v_cvt_pk_bf16_f32 v81, v2, v3\n\t"
    "v_cvt_pk_bf16_f32 v82, v20, v21\n\t"
    "v_cvt_pk_bf16_f32 v83, v22, v23\n\t"
    "v_mfma_f32_16x16x32_bf16 v[48:51], v[140:143], v[124:127], v[48:51]\n\t"
    "v_cvt_pk_bf16_f32 v84, v40, v41\n\t"
    "v_cvt_pk_bf16_f32 v85, v42, v43\n\t"
    "v_cvt_pk_bf16_f32 v86, v60, v61\n\t"
    "v_cvt_pk_bf16_f32 v87, v62, v63\n\t"
    "v_permlane32_swap_b32 v80, v82\n\t"
    "v_permlane32_swap_b32 v81, v83\n\t"
    "v_mfma_f32_16x16x32_bf16 v[68:71], v[140:143], v[128:131], v[68:71]\n\t"
    "v_permlane32_swap_b32 v84, v86\n\t"
    "v_permlane32_swap_b32 v85, v87\n\t"
    "buffer_store_dwordx4 v[80:83], v155, %[rs], %[so] offen offset:0 sc1\n\t"
    "buffer_store_dwordx4 v[84:87], v155, %[rs], %[so] offen offset:1024 sc1\n\t"
    "s_add_u32 %[so], %[so], 81920\n\t"
    "v_mfma_f32_16x16x32_bf16 v[12:15], v[144:147], v[116:119], v[12:15]\n\t"
    "v_pk_mul_f32 v[4:5], v[4:5], %[sc]\n\t"
    "v_pk_mul_f32 v[6:7], v[6:7], %[sc]\n\t"
    "v_pk_mul_f32 v[24:25], v[24:25], %[sc]\n\t"
    "v_pk_mul_f32 v[26:27], v[26:27], %[sc]\n\t"
    "v_pk_mul_f32 v[44:45], v[44:45], %[sc]\n\t"
    "v_pk_mul_f32 v[46:47], v[46:47], %[sc]\n\t"
    "v_mfma_f32_16x16x32_bf16 v[32:35], v[144:147], v[120:123], v[32:35]\n\t"
    "v_pk_mul_f32 v[64:65], v[64:65], %[sc]\n\t"
    "v_pk_mul_f32 v[66:67], v[66:67], %[sc]\n\t"
    "v_cvt_pk_bf16_f32 v88, v4, v5\n\t"
    "v_cvt_pk_bf16_f32 v89, v6, v7\n\t"
    "v_cvt_pk_bf16_f32 v90, v24, v25\n\t"
    "v_cvt_pk_bf16_f32 v91, v26, v27\n\t"
    "v_mfma_f32_16x16x32_bf16 v[52:55], v[144:147], v[124:127], v[52:55]\n\t"
    "v_cvt_pk_bf16_f32 v92, v44, v45\n\t"
    "v_cvt_pk_bf16_f32 v93, v46, v47\n\t"
    "v_cvt_pk_bf16_f32 v94, v64, v65\n\t"
    "v_cvt_pk_bf16_f32 v95, v66, v67\n\t"
    "v_permlane32_swap_b32 v88, v90\n\t"
    "v_permlane32_swap_b32 v89, v91\n\t"
    "v_mfma_f32_16x16x32_bf16 v[72:75], v[144:147], v[128:131], v[72:75]\n\t"
    "v_permlane32_swap_b32 v92, v94\n\t"
    "v_permlane32_swap_b32 v93, v95\n\t"
    "buffer_store_dwordx4 v[88:91], v155, %[rs], %[so] offen offset:0 sc1\n\t"
    "buffer_store_dwordx4 v[92:95], v155, %[rs], %[so] offen offset:1024 sc1\n\t"
    "s_add_u32 %[so], %[so], 81920\n\t"
    "v_mfma_f32_16x16x32_bf16 v[16:19], v[148:151], v[116:119], v[16:19]\n\t"
    "v_pk_mul_f32 v[8:9], v[8:9], %[sc]\n\t"
    "v_pk_mul_f32 v[10:11], v[10:11], %[sc]\n\t"
    "v_pk_mul_f32 v[28:29], v[28:29], %[sc]\n\t"
    "v_pk_mul_f32 v[30:31], v[30:31], %[sc]\n\t"
    "v_pk_mul_f32 v[48:49], v[48:49], %[sc]\n\t"
    "v_pk_mul_f32 v[50:51], v[50:51], %[sc]\n\t"
    "v_mfma_f32_16x16x32_bf16 v[36:39], v[148:151], v[120:123], v[36:39]\n\t"
    "v_pk_mul_f32 v[68:69], v[68:69], %[sc]\n\t"
    "v_pk_mul_f32 v[70:71], v[70:71], %[sc]\n\t"
    "v_cvt_pk_bf16_f32 v96, v8, v9\n\t"
    "v_cvt_pk_bf16_f32 v97, v10, v11\n\t"
    "v_cvt_pk_bf16_f32 v98, v28, v29\n\t"
    "v_cvt_pk_bf16_f32 v99, v30, v31\n\t"
    "v_mfma_f32_16x16x32_bf16 v[56:59], v[148:151], v[124:127], v[56:59]\n\t"
    "v_cvt_pk_bf16_f32 v100, v48, v49\n\t"
    "v_cvt_pk_bf16_f32 v101, v50, v51\n\t"
    "v_cvt_pk_bf16_f32 v102, v68, v69\n\t"
    "v_cvt_pk_bf16_f32 v103, v70, v71\n\t"
    "v_permlane32_swap_b32 v96, v98\n\t"
    "v_permlane32_swap_b32 v97, v99\n\t"
    "v_mfma_f32_16x16x32_bf16 v[76:79], v[148:151], v[128:131], v[76:79]\n\t"
    "v_permlane32_swap_b32 v100, v102\n\t"
    "v_permlane32_swap_b32 v101, v103\n\t"
    "buffer_store_dwordx4 v[96:99], v155, %[rs], %[so] offen offset:0 sc1\n\t"
    "buffer_store_dwordx4 v[100:103], v155, %[rs], %[so] offen offset:1024 sc1\n\t"
    "s_add_u32 %[so], %[so], 81920\n\t"
    "s_nop 3\n\t"
    "v_pk_mul_f32 v[12:13], v[12:13], %[sc]\n\t"
    "v_pk_mul_f32 v[14:15], v[14:15], %[sc]\n\t"
    "v_pk_mul_f32 v[32:33], v[32:33], %[sc]\n\t"
    "v_pk_mul_f32 v[34:35], v[34:35], %[sc]\n\t"
    "v_pk_mul_f32 v[52:53], v[52:53], %[sc]\n\t"
    "v_pk_mul_f32 v[54:55], v[54:55], %[sc]\n\t"
    "v_pk_mul_f32 v[72:73], v[72:73], %[sc]\n\t"
    "v_pk_mul_f32 v[74:75], v[74:75], %[sc]\n\t"
    "v_cvt_pk_bf16_f32 v104, v12, v13\n\t"
    "v_cvt_pk_bf16_f32 v105, v14, v15\n\t"
    "v_cvt_pk_bf16_f32 v106, v32, v33\n\t"
    "v_cvt_pk_bf16_f32 v107, v34, v35\n\t"
    "v_cvt_pk_bf16_f32 v108, v52, v53\n\t"
    "v_cvt_pk_bf16_f32 v109, v54, v55\n\t"
    "v_cvt_pk_bf16_f32 v110, v72, v73\n\t"
    "v_cvt_pk_bf16_f32 v111, v74, v75\n\t"
    "v_permlane32_swap_b32 v104, v106\n\t"
    "v_permlane32_swap_b32 v105, v107\n\t"
    "v_permlane32_swap_b32 v108, v110\n\t"
    "v_permlane32_swap_b32 v109, v111\n\t"
    "buffer_store_dwordx4 v[104:107], v155, %[rs], %[so] offen offset:0 sc1\n\t"
    "buffer_store_dwordx4 v[108:111], v155, %[rs], %[so] offen offset:1024 sc1\n\t"
    "s_add_u32 %[so], %[so], 81920\n\t"
    "v_pk_mul_f32 v[16:17], v[16:17], %[sc]\n\t"
    "v_pk_mul_f32 v[18:19], v[18:19], %[sc]\n\t"
    "v_pk_mul_f32 v[36:37], v[36:37], %[sc]\n\t"
    "v_pk_mul_f32 v[38:39], v[38:39], %[sc]\n\t"
    "v_pk_mul_f32 v[56:57], v[56:57], %[sc]\n\t"
    "v_pk_mul_f32 v[58:59], v[58:59], %[sc]\n\t"
    "v_pk_mul_f32 v[76:77], v[76:77], %[sc]\n\t"
    "v_pk_mul_f32 v[78:79], v[78:79], %[sc]\n\t"
    "v_cvt_pk_bf16_f32 v158, v16, v17\n\t"
    "v_cvt_pk_bf16_f32 v159, v18, v19\n\t"
    "v_cvt_pk_bf16_f32 v160, v56, v57\n\t"
    "v_cvt_pk_bf16_f32 v161, v58, v59\n\t"
    "v_cvt_pk_bf16_f32 v112, v36, v37\n\t"
    "v_cvt_pk_bf16_f32 v113, v38, v39\n\t"
    "v_cvt_pk_bf16_f32 v114, v76, v77\n\t"
    "v_cvt_pk_bf16_f32 v115, v78, v79\n\t"
    "buffer_store_dwordx4 v[158:161], v156, %[rs], %[ob] offen offset:2048 sc1\n\t"
    "buffer_store_dwordx4 v[112:115], v156, %[rs], %[ob] offen offset:2304 sc1\n\t"
    "s_branch Lend_%=\n\t"
    "Lk_%=:\n\t"
    "v_mfma_f32_16x16x32_bf16 v[0:3], v[132:135], v[116:119], v[0:3]\n\t"
    "v_mbcnt_lo_u32_b32 v152, -1, 0\n\t"
    "v_mbcnt_hi_u32_b32 v152, -1, v152\n\t"
    "v_and_b32 v153, 15, v152\n\t"
    "v_lshrrev_b32 v154, 4, v152\n\t"
    "v_mfma_f32_16x16x32_bf16 v[20:23], v[132:135], v[120:123], v[20:23]\n\t"
    "v_and_b32 v155, 1, v154\n\t"
    "v_lshlrev_b32 v155, 9, v155\n\t"
    "v_lshrrev_b32 v157, 5, v152\n\t"
    "v_and_b32 v156, 3, v153\n\t"
    "v_mfma_f32_16x16x32_bf16 v[40:43], v[132:135], v[124:127], v[40:43]\n\t"
    "v_lshrrev_b32 v153, 2, v153\n\t"
    "v_lshl_add_u32 v156, v153, 3, v156\n\t"
    "v_lshlrev_b32 v156, 4, v156\n\t"
    "v_lshl_add_u32 v157, v157, 6, v156\n\t"
    "v_mfma_f32_16x16x32_bf16 v[60:63], v[132:135], v[128:131], v[60:63]\n\t"
    "v_add_u32 v155, v155, v157\n\t"
    "v_mul_u32_u24 v154, 163840, v154\n\t"
    "v_add_u32 v156, v156, v154\n\t"
    "s_mov_b32 %[so], %[ob]\n\t"
    "v_mfma_f32_16x16x32_bf16 v[4:7], v[136:139], v[116:119], v[4:7]\n\t"
    "v_mfma_f32_16x16x32_bf16 v[24:27], v[136:139], v[120:123], v[24:27]\n\t"
    "v_mfma_f32_16x16x32_bf16 v[44:47], v[136:139], v[124:127], v[44:47]\n\t"
    "v_mfma_f32_16x16x32_bf16 v[64:67], v[136:139], v[128:131], v[64:67]\n\t"
    "v_mfma_f32_16x16x32_bf16 v[8:11], v[140:143], v[116:119], v[8:11]\n\t"
    "v_cvt_pk_bf16_f32 v80, v0, v1\n\t"
    "v_cvt_pk_bf16_f32 v81, v2, v3\n\t"
    "v_cvt_pk_bf16_f32 v82, v20, v21\n\t"
    "v_cvt_pk_bf16_f32 v83, v22, v23\n\t"
    "v_mfma_f32_16x16x32_bf16 v[28:31], v[140:143], v[120:123], v[28:31]\n\t"
    "v_cvt_pk_bf16_f32 v84, v40, v41\n\t"
    "v_cvt_pk_bf16_f32 v85, v42, v43\n\t"
    "v_cvt_pk_bf16_f32 v86, v60, v61\n\t"
    "v_cvt_pk_bf16_f32 v87, v62, v63\n\t"
    "v_mfma_f32_16x16x32_bf16 v[48:51], v[140:143], v[124:127], v[48:51]\n\t"
    "v_permlane32_swap_b32 v80, v82\n\t"
    "v_permlane32_swap_b32 v81, v83\n\t"
    "v_permlane32_swap_b32 v84, v86\n\t"
    "v_permlane32_swap_b32 v85, v87\n\t"
    "v_mfma_f32_16x16x32_bf16 v[68:71], v[140:143], v[128:131], v[68:71]\n\t"
    "buffer_store_dwordx4 v[80:83], v155, %[rs], %[so] offen offset:0 sc1\n\t"
    "buffer_store_dwordx4 v[84:87], v155, %[rs], %[so] offen offset:1024 sc1\n\t"
    "s_add_u32 %[so], %[so], 163840\n\t"
    "v_mfma_f32_16x16x32_bf16 v[12:15], v[144:147], v[116:119], v[12:15]\n\t"
    "v_cvt_pk_bf16_f32 v88, v4, v5\n\t"
    "v_cvt_pk_bf16_f32 v89, v6, v7\n\t"
    "v_cvt_pk_bf16_f32 v90, v24, v25\n\t"
    "v_cvt_pk_bf16_f32 v91, v26, v27\n\t"
    "v_mfma_f32_16x16x32_bf16 v[32:35], v[144:147], v[120:123], v[32:35]\n\t"
    "v_cvt_pk_bf16_f32 v92, v44, v45\n\t"
    "v_cvt_pk_bf16_f32 v93, v46, v47\n\t"
    "v_cvt_pk_bf16_f32 v94, v64, v65\n\t"
    "v_cvt_pk_bf16_f32 v95, v66, v67\n\t"
    "v_mfma_f32_16x16x32_bf16 v[52:55], v[144:147], v[124:127], v[52:55]\n\t"
    "v_permlane32_swap_b32 v88, v90\n\t"
    "v_permlane32_swap_b32 v89, v91\n\t"
    "v_permlane32_swap_b32 v92, v94\n\t"
    "v_permlane32_swap_b32 v93, v95\n\t"
    "v_mfma_f32_16x16x32_bf16 v[72:75], v[144:147], v[128:131], v[72:75]\n\t"
    "buffer_store_dwordx4 v[88:91], v155, %[rs], %[so] offen offset:0 sc1\n\t"
    "buffer_store_dwordx4 v[92:95], v155, %[rs], %[so] offen offset:1024 sc1\n\t"
    "s_add_u32 %[so], %[so], 163840\n\t"
    "v_mfma_f32_16x16x32_bf16 v[16:19], v[148:151], v[116:119], v[16:19]\n\t"
    "v_cvt_pk_bf16_f32 v96, v8, v9\n\t"
    "v_cvt_pk_bf16_f32 v97, v10, v11\n\t"
    "v_cvt_pk_bf16_f32 v98, v28, v29\n\t"
    "v_cvt_pk_bf16_f32 v99, v30, v31\n\t"
    "v_mfma_f32_16x16x32_bf16 v[36:39], v[148:151], v[120:123], v[36:39]\n\t"
    "v_cvt_pk_bf16_f32 v100, v48, v49\n\t"
    "v_cvt_pk_bf16_f32 v101, v50, v51\n\t"
    "v_cvt_pk_bf16_f32 v102, v68, v69\n\t"
    "v_cvt_pk_bf16_f32 v103, v70, v71\n\t"
    "v_mfma_f32_16x16x32_bf16 v[56:59], v[148:151], v[124:127], v[56:59]\n\t"
    "v_permlane32_swap_b32 v96, v98\n\t"
    "v_permlane32_swap_b32 v97, v99\n\t"
    "v_permlane32_swap_b32 v100, v102\n\t"
    "v_permlane32_swap_b32 v101, v103\n\t"
    "v_mfma_f32_16x16x32_bf16 v[76:79], v[148:151], v[128:131], v[76:79]\n\t"
    "buffer_store_dwordx4 v[96:99], v155, %[rs], %[so] offen offset:0 sc1\n\t"
    "buffer_store_dwordx4 v[100:103], v155, %[rs], %[so] offen offset:1024 sc1\n\t"
    "s_add_u32 %[so], %[so], 163840\n\t"
    "s_nop 3\n\t"
    "v_cvt_pk_bf16_f32 v104, v12, v13\n\t"
    "v_cvt_pk_bf16_f32 v105, v14, v15\n\t"
    "v_cvt_pk_bf16_f32 v106, v32, v33\n\t"
    "v_cvt_pk_bf16_f32 v107, v34, v35\n\t"
    "v_cvt_pk_bf16_f32 v108, v52, v53\n\t"
    "v_cvt_pk_bf16_f32 v109, v54, v55\n\t"
    "v_cvt_pk_bf16_f32 v110, v72, v73\n\t"
    "v_cvt_pk_bf16_f32 v111, v74, v75\n\t"
    "v_permlane32_swap_b32 v104, v106\n\t"
    "v_permlane32_swap_b32 v105, v107\n\t"
    "v_permlane32_swap_b32 v108, v110\n\t"
    "v_permlane32_swap_b32 v109, v111\n\t"
    "buffer_store_dwordx4 v[104:107], v155, %[rs], %[so] offen offset:0 sc1\n\t"
    "buffer_store_dwordx4 v[108:111], v155, %[rs], %[so] offen offset:1024 sc1\n\t"
    "s_add_u32 %[so], %[so], 163840\n\t"
    "v_cvt_pk_bf16_f32 v158, v16, v17\n\t"
    "v_cvt_pk_bf16_f32 v159, v18, v19\n\t"
    "v_cvt_pk_bf16_f32 v160, v56, v57\n\t"
    "v_cvt_pk_bf16_f32 v161, v58, v59\n\t"
    "v_cvt_pk_bf16_f32 v112, v36, v37\n\t"
    "v_cvt_pk_bf16_f32 v113, v38, v39\n\t"
    "v_cvt_pk_bf16_f32 v114, v76, v77\n\t"
    "v_cvt_pk_bf16_f32 v115, v78, v79\n\t"
    "buffer_store_dwordx4 v[158:161], v156, %[rs], %[ob] offen offset:2048 sc1\n\t"
    "buffer_store_dwordx4 v[112:115], v156, %[rs], %[ob] offen offset:2112 sc1\n\t"
    "s_branch Lend_%=\n\t"
    "Lxa_%=:\n\t"
    "s_barrier\n\t"
    "ds_read_b64_tr_b16 v[96:97], v153 offset:0\n\t"
    "ds_read_b64_tr_b16 v[98:99], v153 offset:512\n\t"
    "ds_read_b128 v[80:83], v152 offset:0\n\t"
    "ds_read_b128 v[84:87], v152 offset:1024\n\t"
    "ds_read_b128 v[88:91], v152 offset:2048\n\t"
    "ds_read_b128 v[92:95], v152 offset:3072\n\t"
    "ds_read_b64_tr_b16 v[100:101], v154 offset:0\n\t"
    "ds_read_b64_tr_b16 v[102:103], v154 offset:512\n\t"
    "ds_read_b64_tr_b16 v[104:105], v155 offset:0\n\t"
    "ds_read_b64_tr_b16 v[106:107], v155 offset:512\n\t"
    "ds_read_b64_tr_b16 v[108:109], v156 offset:0\n\t"
    "ds_read_b64_tr_b16 v[110:111], v156 offset:512\n\t"
    "ds_read_b64_tr_b16 v[112:113], v157 offset:0\n\t"
    "ds_read_b64_tr_b16 v[114:115], v157 offset:512\n\t"
    "s_waitcnt lgkmcnt(0)\n\t"
    "v_mfma_f32_16x16x32_bf16 v[0:3], v[80:83], v[96:99], 0\n\t"
    "v_mfma_f32_16x16x32_bf16 v[20:23], v[84:87], v[96:99], 0\n\t"
    "s_barrier\n\t"
    "v_mfma_f32_16x16x32_bf16 v[40:43], v[88:91], v[96:99], 0\n\t"
    "ds_read_b64_tr_b16 v[132:133], v153 offset:20480\n\t"
    "v_mfma_f32_16x16x32_bf16 v[60:63], v[92:95], v[96:99], 0\n\t"
    "ds_read_b64_tr_b16 v[134:135], v153 offset:20992\n\t"
    "v_mfma_f32_16x16x32_bf16 v[64:67], v[92:95], v[100:103], 0\n\t"
    "ds_read_b128 v[116:119], v152 offset:8192\n\t"
    "v_mfma_f32_16x16x32_bf16 v[44:47], v[88:91], v[100:103], 0\n\t"
    "ds_read_b128 v[120:123], v152 offset:9216\n\t"
    "v_mfma_f32_16x16x32_bf16 v[24:27], v[84:87], v[100:103], 0\n\t"
    "ds_read_b128 v[124:127], v152 offset:10240\n\t"
    "v_mfma_f32_16x16x32_bf16 v[4:7], v[80:83], v[100:103], 0\n\t"
    "ds_read_b128 v[128:131], v152 offset:11264\n\t"
    "v_mfma_f32_16x16x32_bf16 v[8:11], v[80:83], v[104:107], 0\n\t"
    "ds_read_b64_tr_b16 v[136:137], v154 offset:20480\n\t"
    "v_mfma_f32_16x16x32_bf16 v[28:31], v[84:87], v[104:107], 0\n\t"
    "ds_read_b64_tr_b16 v[138:139], v154 offset:20992\n\t"
    "v_mfma_f32_16x16x32_bf16 v[48:51], v[88:91], v[104:107], 0\n\t"
    "ds_read_b64_tr_b16 v[140:141], v155 offset:20480\n\t"
    "v_mfma_f32_16x16x32_bf16 v[68:71], v[92:95], v[104:107], 0\n\t"
    "ds_read_b64_tr_b16 v[142:143], v155 offset:20992\n\t"
    "v_mfma_f32_16x16x32_bf16 v[72:75], v[92:95], v[108:111], 0\n\t"
    "ds_read_b64_tr_b16 v[144:145], v156 offset:20480\n\t"
    "v_mfma_f32_16x16x32_bf16 v[52:55], v[88:91], v[108:111], 0\n\t"
    "ds_read_b64_tr_b16 v[146:147], v156 offset:20992\n\t"
    "v_mfma_f32_16x16x32_bf16 v[32:35], v[84:87], v[108:111], 0\n\t"
    "ds_read_b64_tr_b16 v[148:149], v157 offset:20480\n\t"
    "v_mfma_f32_16x16x32_bf16 v[12:15], v[80:83], v[108:111], 0\n\t"
    "ds_read_b64_tr_b16 v[150:151], v157 offset:20992\n\t"
    "v_mfma_f32_16x16x32_bf16 v[16:19], v[80:83], v[112:115], 0\n\t"
    "v_mfma_f32_16x16x32_bf16 v[36:39], v[84:87], v[112:115], 0\n\t"
    "v_mfma_f32_16x16x32_bf16 v[56:59], v[88:91], v[112:115], 0\n\t"
    "v_mfma_f32_16x16x32_bf16 v[76:79], v[92:95], v[112:115], 0\n\t"
    "s_waitcnt lgkmcnt(0)\n\t"
    "v_mfma_f32_16x16x32_bf16 v[0:3], v[116:119], v[132:135], v[0:3]\n\t"
    "v_mfma_f32_16x16x32_bf16 v[20:23], v[120:123], v[132:135], v[20:23]\n\t"
    "s_barrier\n\t"
    "v_mfma_f32_16x16x32_bf16 v[40:43], v[124:127], v[132:135], v[40:43]\n\t"
    "ds_read_b64_tr_b16 v[96:97], v153 offset:40960\n\t"
    "v_mfma_f32_16x16x32_bf16 v[60:63], v[128:131], v[132:135], v[60:63]\n\t"
    "ds_read_b64_tr_b16 v[98:99], v153 offset:41472\n\t"
    "v_mfma_f32_16x16x32_bf16 v[64:67], v[128:131], v[136:139], v[64:67]\n\t"
    "ds_read_b128 v[80:83], v152 offset:16384\n\t"
    "v_mfma_f32_16x16x32_bf16 v[44:47], v[124:127], v[136:139], v[44:47]\n\t"
    "ds_read_b128 v[84:87], v152 offset:17408\n\t"
    "v_mfma_f32_16x16x32_bf16 v[24:27], v[120:123], v[136:139], v[24:27]\n\t"
    "ds_read_b128 v[88:91], v152 offset:18432\n\t"
    "v_mfma_f32_16x16x32_bf16 v[4:7], v[116:119], v[136:139], v[4:7]\n\t"
    "ds_read_b128 v[92:95], v152 offset:19456\n\t"
    "v_mfma_f32_16x16x32_bf16 v[8:11], v[116:119], v[140:143], v[8:11]\n\t"
    "ds_read_b64_tr_b16 v[100:101], v154 offset:40960\n\t"
    "v_mfma_f32_16x16x32_bf16 v[28:31], v[120:123], v[140:143], v[28:31]\n\t"
    "ds_read_b64_tr_b16 v[102:103], v154 offset:41472\n\t"
    "v_mfma_f32_16x16x32_bf16 v[48:51], v[124:127], v[140:143], v[48:51]\n\t"
    "ds_read_b64_tr_b16 v[104:105], v155 offset:40960\n\t"
    "v_mfma_f32_16x16x32_bf16 v[68:71], v[128:131], v[140:143], v[68:71]\n\t"
    "ds_read_b64_tr_b16 v[106:107], v155 offset:41472\n\t"
    "v_mfma_f32_16x16x32_bf16 v[72:75], v[128:131], v[144:147], v[72:75]\n\t"
    "ds_read_b64_tr_b16 v[108:109], v156 offset:40960\n\t"
    "v_mfma_f32_16x16x32_bf16 v[52:55], v[124:127], v[144:147], v[52:55]\n\t"
    "ds_read_b64_tr_b16 v[110:111], v156 offset:41472\n\t"
    "v_mfma_f32_16x16x32_bf16 v[32:35], v[120:123], v[144:147], v[32:35]\n\t"
    "ds_read_b64_tr_b16 v[112:113], v157 offset:40960\n\t"
    "v_mfma_f32_16x16x32_bf16 v[12:15], v[116:119], v[144:147], v[12:15]\n\t"
    "ds_read_b64_tr_b16 v[114:115], v157 offset:41472\n\t"
    "v_mfma_f32_16x16x32_bf16 v[16:19], v[116:119], v[148:151], v[16:19]\n\t"
    "v_mfma_f32_16x16x32_bf16 v[36:39], v[120:123], v[148:151], v[36:39]\n\t"
    "v_mfma_f32_16x16x32_bf16 v[56:59], v[124:127], v[148:151], v[56:59]\n\t"
    "v_mfma_f32_16x16x32_bf16 v[76:79], v[128:131], v[148:151], v[76:79]\n\t"
    "s_waitcnt lgkmcnt(0)\n\t"
    "v_mfma_f32_16x16x32_bf16 v[0:3], v[80:83], v[96:99], v[0:3]\n\t"
    "v_mfma_f32_16x16x32_bf16 v[20:23], v[84:87], v[96:99], v[20:23]\n\t"
    "s_barrier\n\t"
    "v_mfma_f32_16x16x32_bf16 v[40:43], v[88:91], v[96:99], v[40:43]\n\t"
    "ds_read_b64_tr_b16 v[132:133], v158 offset:0\n\t"
    "v_mfma_f32_16x16x32_bf16 v[60:63], v[92:95], v[96:99], v[60:63]\n\t"
    "ds_read_b64_tr_b16 v[134:135], v158 offset:512\n\t"
    "v_mfma_f32_16x16x32_bf16 v[64:67], v[92:95], v[100:103], v[64:67]\n\t"
    "ds_read_b128 v[116:119], v152 offset:24576\n\t"
    "v_mfma_f32_16x16x32_bf16 v[44:47], v[88:91], v[100:103], v[44:47]\n\t"
    "ds_read_b128 v[120:123], v152 offset:25600\n\t"
    "v_mfma_f32_16x16x32_bf16 v[24:27], v[84:87], v[100:103], v[24:27]\n\t"
    "ds_read_b128 v[124:127], v152 offset:26624\n\t"
    "v_mfma_f32_16x16x32_bf16 v[4:7], v[80:83], v[100:103], v[4:7]\n\t"
    "ds_read_b128 v[128:131], v152 offset:27648\n\t"
    "v_mfma_f32_16x16x32_bf16 v[8:11], v[80:83], v[104:107], v[8:11]\n\t"
    "ds_read_b64_tr_b16 v[136:137], v159 offset:0\n\t"
    "v_mfma_f32_16x16x32_bf16 v[28:31], v[84:87], v[104:107], v[28:31]\n\t"
    "ds_read_b64_tr_b16 v[138:139], v159 offset:512\n\t"
    "v_mfma_f32_16x16x32_bf16 v[48:51], v[88:91], v[104:107], v[48:51]\n\t"
    "ds_read_b64_tr_b16 v[140:141], v160 offset:0\n\t"
    "v_mfma_f32_16x16x32_bf16 v[68:71], v[92:95], v[104:107], v[68:71]\n\t"
    "ds_read_b64_tr_b16 v[142:143], v160 offset:512\n\t"
    "v_mfma_f32_16x16x32_bf16 v[72:75], v[92:95], v[108:111], v[72:75]\n\t"
    "ds_read_b64_tr_b16 v[144:145], v161 offset:0\n\t"
    "v_mfma_f32_16x16x32_bf16 v[52:55], v[88:91], v[108:111], v[52:55]\n\t"
    "ds_read_b64_tr_b16 v[146:147], v161 offset:512\n\t"
    "v_mfma_f32_16x16x32_bf16 v[32:35], v[84:87], v[108:111], v[32:35]\n\t"
    "ds_read_b64_tr_b16 v[148:149], v162 offset:0\n\t"
    "v_mfma_f32_16x16x32_bf16 v[12:15], v[80:83], v[108:111], v[12:15]\n\t"
    "ds_read_b64_tr_b16 v[150:151], v162 offset:512\n\t"
    "v_mfma_f32_16x16x32_bf16 v[16:19], v[80:83], v[112:115], v[16:19]\n\t"
    "v_mfma_f32_16x16x32_bf16 v[36:39], v[84:87], v[112:115], v[36:39]\n\t"
    "v_mfma_f32_16x16x32_bf16 v[56:59], v[88:91], v[112:115], v[56:59]\n\t"
    "v_mfma_f32_16x16x32_bf16 v[76:79], v[92:95], v[112:115], v[76:79]\n\t"
    "s_waitcnt lgkmcnt(0)\n\t"
    "v_mfma_f32_16x16x32_bf16 v[0:3], v[116:119], v[132:135], v[0:3]\n\t"
    "v_mfma_f32_16x16x32_bf16 v[20:23], v[120:123], v[132:135], v[20:23]\n\t"
    "s_barrier\n\t"
    "v_mfma_f32_16x16x32_bf16 v[40:43], v[124:127], v[132:135], v[40:43]\n\t"
    "ds_read_b64_tr_b16 v[96:97], v158 offset:20480\n\t"
    "v_mfma_f32_16x16x32_bf16 v[60:63], v[128:131], v[132:135], v[60:63]\n\t"
    "ds_read_b64_tr_b16 v[98:99], v158 offset:20992\n\t"
    "v_mfma_f32_16x16x32_bf16 v[64:67], v[128:131], v[136:139], v[64:67]\n\t"
    "ds_read_b128 v[80:83], v152 offset:32768\n\t"
    "v_mfma_f32_16x16x32_bf16 v[44:47], v[124:127], v[136:139], v[44:47]\n\t"
    "ds_read_b128 v[84:87], v152 offset:33792\n\t"
    "v_mfma_f32_16x16x32_bf16 v[24:27], v[120:123], v[136:139], v[24:27]\n\t"
    "ds_read_b128 v[88:91], v152 offset:34816\n\t"
    "v_mfma_f32_16x16x32_bf16 v[4:7], v[116:119], v[136:139], v[4:7]\n\t"
    "ds_read_b128 v[92:95], v152 offset:35840\n\t"
    "v_mfma_f32_16x16x32_bf16 v[8:11], v[116:119], v[140:143], v[8:11]\n\t"
    "ds_read_b64_tr_b16 v[100:101], v159 offset:20480\n\t"
    "v_mfma_f32_16x16x32_bf16 v[28:31], v[120:123], v[140:143], v[28:31]\n\t"
    "ds_read_b64_tr_b16 v[102:103], v159 offset:20992\n\t"
    "v_mfma_f32_16x16x32_bf16 v[48:51], v[124:127], v[140:143], v[48:51]\n\t"
    "ds_read_b64_tr_b16 v[104:105], v160 offset:20480\n\t"
    "v_mfma_f32_16x16x32_bf16 v[68:71], v[128:131], v[140:143], v[68:71]\n\t"
    "ds_read_b64_tr_b16 v[106:107], v160 offset:20992\n\t"
    "v_mfma_f32_16x16x32_bf16 v[72:75], v[128:131], v[144:147], v[72:75]\n\t"
    "ds_read_b64_tr_b16 v[108:109], v161 offset:20480\n\t"
    "v_mfma_f32_16x16x32_bf16 v[52:55], v[124:127], v[144:147], v[52:55]\n\t"
    "ds_read_b64_tr_b16 v[110:111], v161 offset:20992\n\t"
    "v_mfma_f32_16x16x32_bf16 v[32:35], v[120:123], v[144:147], v[32:35]\n\t"
    "ds_read_b64_tr_b16 v[112:113], v162 offset:20480\n\t"
    "v_mfma_f32_16x16x32_bf16 v[12:15], v[116:119], v[144:147], v[12:15]\n\t"
    "ds_read_b64_tr_b16 v[114:115], v162 offset:20992\n\t"
    "v_mfma_f32_16x16x32_bf16 v[16:19], v[116:119], v[148:151], v[16:19]\n\t"
    "v_mfma_f32_16x16x32_bf16 v[36:39], v[120:123], v[148:151], v[36:39]\n\t"
    "v_mfma_f32_16x16x32_bf16 v[56:59], v[124:127], v[148:151], v[56:59]\n\t"
    "v_mfma_f32_16x16x32_bf16 v[76:79], v[128:131], v[148:151], v[76:79]\n\t"
    "s_waitcnt lgkmcnt(0)\n\t"
    "v_mfma_f32_16x16x32_bf16 v[0:3], v[80:83], v[96:99], v[0:3]\n\t"
    "v_mfma_f32_16x16x32_bf16 v[20:23], v[84:87], v[96:99], v[20:23]\n\t"
    "s_barrier\n\t"
    "v_mfma_f32_16x16x32_bf16 v[40:43], v[88:91], v[96:99], v[40:43]\n\t"
    "ds_read_b64_tr_b16 v[132:133], v153 offset:0\n\t"
    "v_mfma_f32_16x16x32_bf16 v[60:63], v[92:95], v[96:99], v[60:63]\n\t"
    "ds_read_b64_tr_b16 v[134:135], v153 offset:512\n\t"
    "v_mfma_f32_16x16x32_bf16 v[64:67], v[92:95], v[100:103], v[64:67]\n\t"
    "ds_read_b128 v[116:119], v152 offset:0\n\t"
    "v_mfma_f32_16x16x32_bf16 v[44:47], v[88:91], v[100:103], v[44:47]\n\t"
    "ds_read_b128 v[120:123], v152 offset:1024\n\t"
    "v_mfma_f32_16x16x32_bf16 v[24:27], v[84:87], v[100:103], v[24:27]\n\t"
    "ds_read_b128 v[124:127], v152 offset:2048\n\t"
    "v_mfma_f32_16x16x32_bf16 v[4:7], v[80:83], v[100:103], v[4:7]\n\t"
    "ds_read_b128 v[128:131], v152 offset:3072\n\t"
    "v_mfma_f32_16x16x32_bf16 v[8:11], v[80:83], v[104:107], v[8:11]\n\t"
    "ds_read_b64_tr_b16 v[136:137], v154 offset:0\n\t"
    "v_mfma_f32_16x16x32_bf16 v[28:31], v[84:87], v[104:107], v[28:31]\n\t"
    "ds_read_b64_tr_b16 v[138:139], v154 offset:512\n\t"
    "v_mfma_f32_16x16x32_bf16 v[48:51], v[88:91], v[104:107], v[48:51]\n\t"
    "ds_read_b64_tr_b16 v[140:141], v155 offset:0\n\t"
    "v_mfma_f32_16x16x32_bf16 v[68:71], v[92:95], v[104:107], v[68:71]\n\t"
    "ds_read_b64_tr_b16 v[142:143], v155 offset:512\n\t"
    "v_mfma_f32_16x16x32_bf16 v[72:75], v[92:95], v[108:111], v[72:75]\n\t"
    "ds_read_b64_tr_b16 v[144:145], v156 offset:0\n\t"
    "v_mfma_f32_16x16x32_bf16 v[52:55], v[88:91], v[108:111], v[52:55]\n\t"
    "ds_read_b64_tr_b16 v[146:147], v156 offset:512\n\t"
    "v_mfma_f32_16x16x32_bf16 v[32:35], v[84:87], v[108:111], v[32:35]\n\t"
    "ds_read_b64_tr_b16 v[148:149], v157 offset:0\n\t"
    "v_mfma_f32_16x16x32_bf16 v[12:15], v[80:83], v[108:111], v[12:15]\n\t"
    "ds_read_b64_tr_b16 v[150:151], v157 offset:512\n\t"
    "v_mfma_f32_16x16x32_bf16 v[16:19], v[80:83], v[112:115], v[16:19]\n\t"
    "v_mfma_f32_16x16x32_bf16 v[36:39], v[84:87], v[112:115], v[36:39]\n\t"
    "v_mfma_f32_16x16x32_bf16 v[56:59], v[88:91], v[112:115], v[56:59]\n\t"
    "v_mfma_f32_16x16x32_bf16 v[76:79], v[92:95], v[112:115], v[76:79]\n\t"
    "s_waitcnt lgkmcnt(0)\n\t"
    "v_mfma_f32_16x16x32_bf16 v[0:3], v[116:119], v[132:135], v[0:3]\n\t"
    "v_mfma_f32_16x16x32_bf16 v[20:23], v[120:123], v[132:135], v[20:23]\n\t"
    "s_barrier\n\t"
    "v_mfma_f32_16x16x32_bf16 v[40:43], v[124:127], v[132:135], v[40:43]\n\t"
    "ds_read_b64_tr_b16 v[96:97], v153 offset:20480\n\t"
    "v_mfma_f32_16x16x32_bf16 v[60:63], v[128:131], v[132:135], v[60:63]\n\t"
    "ds_read_b64_tr_b16 v[98:99], v153 offset:20992\n\t"
    "v_mfma_f32_16x16x32_bf16 v[64:67], v[128:131], v[136:139], v[64:67]\n\t"
    "ds_read_b128 v[80:83], v152 offset:8192\n\t"
    "v_mfma_f32_16x16x32_bf16 v[44:47], v[124:127], v[136:139], v[44:47]\n\t"
    "ds_read_b128 v[84:87], v152 offset:9216\n\t"
    "v_mfma_f32_16x16x32_bf16 v[24:27], v[120:123], v[136:139], v[24:27]\n\t"
    "ds_read_b128 v[88:91], v152 offset:10240\n\t"
    "v_mfma_f32_16x16x32_bf16 v[4:7], v[116:119], v[136:139], v[4:7]\n\t"
    "ds_read_b128 v[92:95], v152 offset:11264\n\t"
    "v_mfma_f32_16x16x32_bf16 v[8:11], v[116:119], v[140:143], v[8:11]\n\t"
    "ds_read_b64_tr_b16 v[100:101], v154 offset:20480\n\t"
    "v_mfma_f32_16x16x32_bf16 v[28:31], v[120:123], v[140:143], v[28:31]\n\t"
    "ds_read_b64_tr_b16 v[102:103], v154 offset:20992\n\t"
    "v_mfma_f32_16x16x32_bf16 v[48:51], v[124:127], v[140:143], v[48:51]\n\t"
    "ds_read_b64_tr_b16 v[104:105], v155 offset:20480\n\t"
    "v_mfma_f32_16x16x32_bf16 v[68:71], v[128:131], v[140:143], v[68:71]\n\t"
    "ds_read_b64_tr_b16 v[106:107], v155 offset:20992\n\t"
    "v_mfma_f32_16x16x32_bf16 v[72:75], v[128:131], v[144:147], v[72:75]\n\t"
    "ds_read_b64_tr_b16 v[108:109], v156 offset:20480\n\t"
    "v_mfma_f32_16x16x32_bf16 v[52:55], v[124:127], v[144:147], v[52:55]\n\t"
    "ds_read_b64_tr_b16 v[110:111], v156 offset:20992\n\t"
    "v_mfma_f32_16x16x32_bf16 v[32:35], v[120:123], v[144:147], v[32:35]\n\t"
    "ds_read_b64_tr_b16 v[112:113], v157 offset:20480\n\t"
    "v_mfma_f32_16x16x32_bf16 v[12:15], v[116:119], v[144:147], v[12:15]\n\t"
    "ds_read_b64_tr_b16 v[114:115], v157 offset:20992\n\t"
    "v_mfma_f32_16x16x32_bf16 v[16:19], v[116:119], v[148:151], v[16:19]\n\t"
    "v_mfma_f32_16x16x32_bf16 v[36:39], v[120:123], v[148:151], v[36:39]\n\t"
    "v_mfma_f32_16x16x32_bf16 v[56:59], v[124:127], v[148:151], v[56:59]\n\t"
    "v_mfma_f32_16x16x32_bf16 v[76:79], v[128:131], v[148:151], v[76:79]\n\t"
    "s_waitcnt lgkmcnt(0)\n\t"
    "v_mfma_f32_16x16x32_bf16 v[0:3], v[80:83], v[96:99], v[0:3]\n\t"
    "v_mfma_f32_16x16x32_bf16 v[20:23], v[84:87], v[96:99], v[20:23]\n\t"
    "s_barrier\n\t"
    "v_mfma_f32_16x16x32_bf16 v[40:43], v[88:91], v[96:99], v[40:43]\n\t"
    "ds_read_b64_tr_b16 v[132:133], v153 offset:40960\n\t"
    "v_mfma_f32_16x16x32_bf16 v[60:63], v[92:95], v[96:99], v[60:63]\n\t"
    "ds_read_b64_tr_b16 v[134:135], v153 offset:41472\n\t"
    "v_mfma_f32_16x16x32_bf16 v[64:67], v[92:95], v[100:103], v[64:67]\n\t"
    "ds_read_b128 v[116:119], v152 offset:16384\n\t"
    "v_mfma_f32_16x16x32_bf16 v[44:47], v[88:91], v[100:103], v[44:47]\n\t"
    "ds_read_b128 v[120:123], v152 offset:17408\n\t"
    "v_mfma_f32_16x16x32_bf16 v[24:27], v[84:87], v[100:103], v[24:27]\n\t"
    "ds_read_b128 v[124:127], v152 offset:18432\n\t"
    "v_mfma_f32_16x16x32_bf16 v[4:7], v[80:83], v[100:103], v[4:7]\n\t"
    "ds_read_b128 v[128:131], v152 offset:19456\n\t"
    "v_mfma_f32_16x16x32_bf16 v[8:11], v[80:83], v[104:107], v[8:11]\n\t"
    "ds_read_b64_tr_b16 v[136:137], v154 offset:40960\n\t"
    "v_mfma_f32_16x16x32_bf16 v[28:31], v[84:87], v[104:107], v[28:31]\n\t"
    "ds_read_b64_tr_b16 v[138:139], v154 offset:41472\n\t"
    "v_mfma_f32_16x16x32_bf16 v[48:51], v[88:91], v[104:107], v[48:51]\n\t"
    "ds_read_b64_tr_b16 v[140:141], v155 offset:40960\n\t"
    "v_mfma_f32_16x16x32_bf16 v[68:71], v[92:95], v[104:107], v[68:71]\n\t"
    "ds_read_b64_tr_b16 v[142:143], v155 offset:41472\n\t"
    "v_mfma_f32_16x16x32_bf16 v[72:75], v[92:95], v[108:111], v[72:75]\n\t"
    "ds_read_b64_tr_b16 v[144:145], v156 offset:40960\n\t"
    "v_mfma_f32_16x16x32_bf16 v[52:55], v[88:91], v[108:111], v[52:55]\n\t"
    "ds_read_b64_tr_b16 v[146:147], v156 offset:41472\n\t"
    "v_mfma_f32_16x16x32_bf16 v[32:35], v[84:87], v[108:111], v[32:35]\n\t"
    "ds_read_b64_tr_b16 v[148:149], v157 offset:40960\n\t"
    "v_mfma_f32_16x16x32_bf16 v[12:15], v[80:83], v[108:111], v[12:15]\n\t"
    "ds_read_b64_tr_b16 v[150:151], v157 offset:41472\n\t"
    "v_mfma_f32_16x16x32_bf16 v[16:19], v[80:83], v[112:115], v[16:19]\n\t"
    "v_mfma_f32_16x16x32_bf16 v[36:39], v[84:87], v[112:115], v[36:39]\n\t"
    "v_mfma_f32_16x16x32_bf16 v[56:59], v[88:91], v[112:115], v[56:59]\n\t"
    "v_mfma_f32_16x16x32_bf16 v[76:79], v[92:95], v[112:115], v[76:79]\n\t"
    "s_waitcnt lgkmcnt(0)\n\t"
    "v_mfma_f32_16x16x32_bf16 v[0:3], v[116:119], v[132:135], v[0:3]\n\t"
    "v_mfma_f32_16x16x32_bf16 v[20:23], v[120:123], v[132:135], v[20:23]\n\t"
    "s_barrier\n\t"
    "v_mfma_f32_16x16x32_bf16 v[40:43], v[124:127], v[132:135], v[40:43]\n\t"
    "ds_read_b64_tr_b16 v[96:97], v158 offset:0\n\t"
    "v_mfma_f32_16x16x32_bf16 v[60:63], v[128:131], v[132:135], v[60:63]\n\t"
    "ds_read_b64_tr_b16 v[98:99], v158 offset:512\n\t"
    "v_mfma_f32_16x16x32_bf16 v[64:67], v[128:131], v[136:139], v[64:67]\n\t"
    "ds_read_b128 v[80:83], v152 offset:24576\n\t"
    "v_mfma_f32_16x16x32_bf16 v[44:47], v[124:127], v[136:139], v[44:47]\n\t"
    "ds_read_b128 v[84:87], v152 offset:25600\n\t"
    "v_mfma_f32_16x16x32_bf16 v[24:27], v[120:123], v[136:139], v[24:27]\n\t"
    "ds_read_b128 v[88:91], v152 offset:26624\n\t"
    "v_mfma_f32_16x16x32_bf16 v[4:7], v[116:119], v[136:139], v[4:7]\n\t"
    "ds_read_b128 v[92:95], v152 offset:27648\n\t"
    "v_mfma_f32_16x16x32_bf16 v[8:11], v[116:119], v[140:143], v[8:11]\n\t"
    "ds_read_b64_tr_b16 v[100:101], v159 offset:0\n\t"
    "v_mfma_f32_16x16x32_bf16 v[28:31], v[120:123], v[140:143], v[28:31]\n\t"
    "ds_read_b64_tr_b16 v[102:103], v159 offset:512\n\t"
    "v_mfma_f32_16x16x32_bf16 v[48:51], v[124:127], v[140:143], v[48:51]\n\t"
    "ds_read_b64_tr_b16 v[104:105], v160 offset:0\n\t"
    "v_mfma_f32_16x16x32_bf16 v[68:71], v[128:131], v[140:143], v[68:71]\n\t"
    "ds_read_b64_tr_b16 v[106:107], v160 offset:512\n\t"
    "v_mfma_f32_16x16x32_bf16 v[72:75], v[128:131], v[144:147], v[72:75]\n\t"
    "ds_read_b64_tr_b16 v[108:109], v161 offset:0\n\t"
    "v_mfma_f32_16x16x32_bf16 v[52:55], v[124:127], v[144:147], v[52:55]\n\t"
    "ds_read_b64_tr_b16 v[110:111], v161 offset:512\n\t"
    "v_mfma_f32_16x16x32_bf16 v[32:35], v[120:123], v[144:147], v[32:35]\n\t"
    "ds_read_b64_tr_b16 v[112:113], v162 offset:0\n\t"
    "v_mfma_f32_16x16x32_bf16 v[12:15], v[116:119], v[144:147], v[12:15]\n\t"
    "ds_read_b64_tr_b16 v[114:115], v162 offset:512\n\t"
    "v_mfma_f32_16x16x32_bf16 v[16:19], v[116:119], v[148:151], v[16:19]\n\t"
    "v_mfma_f32_16x16x32_bf16 v[36:39], v[120:123], v[148:151], v[36:39]\n\t"
    "v_mfma_f32_16x16x32_bf16 v[56:59], v[124:127], v[148:151], v[56:59]\n\t"
    "v_mfma_f32_16x16x32_bf16 v[76:79], v[128:131], v[148:151], v[76:79]\n\t"
    "s_waitcnt lgkmcnt(0)\n\t"
    "v_mfma_f32_16x16x32_bf16 v[0:3], v[80:83], v[96:99], v[0:3]\n\t"
    "v_mfma_f32_16x16x32_bf16 v[20:23], v[84:87], v[96:99], v[20:23]\n\t"
    "s_barrier\n\t"
    "v_mfma_f32_16x16x32_bf16 v[40:43], v[88:91], v[96:99], v[40:43]\n\t"
    "ds_read_b64_tr_b16 v[132:133], v158 offset:20480\n\t"
    "v_mfma_f32_16x16x32_bf16 v[60:63], v[92:95], v[96:99], v[60:63]\n\t"
    "ds_read_b64_tr_b16 v[134:135], v158 offset:20992\n\t"
    "v_mfma_f32_16x16x32_bf16 v[64:67], v[92:95], v[100:103], v[64:67]\n\t"
    "ds_read_b128 v[116:119], v152 offset:32768\n\t"
    "v_mfma_f32_16x16x32_bf16 v[44:47], v[88:91], v[100:103], v[44:47]\n\t"
    "ds_read_b128 v[120:123], v152 offset:33792\n\t"
    "v_mfma_f32_16x16x32_bf16 v[24:27], v[84:87], v[100:103], v[24:27]\n\t"
    "ds_read_b128 v[124:127], v152 offset:34816\n\t"
    "v_mfma_f32_16x16x32_bf16 v[4:7], v[80:83], v[100:103], v[4:7]\n\t"
    "ds_read_b128 v[128:131], v152 offset:35840\n\t"
    "v_mfma_f32_16x16x32_bf16 v[8:11], v[80:83], v[104:107], v[8:11]\n\t"
    "ds_read_b64_tr_b16 v[136:137], v159 offset:20480\n\t"
    "v_mfma_f32_16x16x32_bf16 v[28:31], v[84:87], v[104:107], v[28:31]\n\t"
    "ds_read_b64_tr_b16 v[138:139], v159 offset:20992\n\t"
    "v_mfma_f32_16x16x32_bf16 v[48:51], v[88:91], v[104:107], v[48:51]\n\t"
    "ds_read_b64_tr_b16 v[140:141], v160 offset:20480\n\t"
    "v_mfma_f32_16x16x32_bf16 v[68:71], v[92:95], v[104:107], v[68:71]\n\t"
    "ds_read_b64_tr_b16 v[142:143], v160 offset:20992\n\t"
    "v_mfma_f32_16x16x32_bf16 v[72:75], v[92:95], v[108:111], v[72:75]\n\t"
    "ds_read_b64_tr_b16 v[144:145], v161 offset:20480\n\t"
    "v_mfma_f32_16x16x32_bf16 v[52:55], v[88:91], v[108:111], v[52:55]\n\t"
    "ds_read_b64_tr_b16 v[146:147], v161 offset:20992\n\t"
    "v_mfma_f32_16x16x32_bf16 v[32:35], v[84:87], v[108:111], v[32:35]\n\t"
    "ds_read_b64_tr_b16 v[148:149], v162 offset:20480\n\t"
    "v_mfma_f32_16x16x32_bf16 v[12:15], v[80:83], v[108:111], v[12:15]\n\t"
    "ds_read_b64_tr_b16 v[150:151], v162 offset:20992\n\t"
    "v_mfma_f32_16x16x32_bf16 v[16:19], v[80:83], v[112:115], v[16:19]\n\t"
    "v_mfma_f32_16x16x32_bf16 v[36:39], v[84:87], v[112:115], v[36:39]\n\t"
    "v_mfma_f32_16x16x32_bf16 v[56:59], v[88:91], v[112:115], v[56:59]\n\t"
    "v_mfma_f32_16x16x32_bf16 v[76:79], v[92:95], v[112:115], v[76:79]\n\t"
    "s_waitcnt lgkmcnt(0)\n\t"
    "v_mfma_f32_16x16x32_bf16 v[0:3], v[116:119], v[132:135], v[0:3]\n\t"
    "v_mfma_f32_16x16x32_bf16 v[20:23], v[120:123], v[132:135], v[20:23]\n\t"
    "s_barrier\n\t"
    "v_mfma_f32_16x16x32_bf16 v[40:43], v[124:127], v[132:135], v[40:43]\n\t"
    "ds_read_b64_tr_b16 v[96:97], v153 offset:0\n\t"
    "v_mfma_f32_16x16x32_bf16 v[60:63], v[128:131], v[132:135], v[60:63]\n\t"
    "ds_read_b64_tr_b16 v[98:99], v153 offset:512\n\t"
    "v_mfma_f32_16x16x32_bf16 v[64:67], v[128:131], v[136:139], v[64:67]\n\t"
    "ds_read_b128 v[80:83], v152 offset:0\n\t"
    "v_mfma_f32_16x16x32_bf16 v[44:47], v[124:127], v[136:139], v[44:47]\n\t"
    "ds_read_b128 v[84:87], v152 offset:1024\n\t"
    "v_mfma_f32_16x16x32_bf16 v[24:27], v[120:123], v[136:139], v[24:27]\n\t"
    "ds_read_b128 v[88:91], v152 offset:2048\n\t"
    "v_mfma_f32_16x16x32_bf16 v[4:7], v[116:119], v[136:139], v[4:7]\n\t"
    "ds_read_b128 v[92:95], v152 offset:3072\n\t"
    "v_mfma_f32_16x16x32_bf16 v[8:11], v[116:119], v[140:143], v[8:11]\n\t"
    "ds_read_b64_tr_b16 v[100:101], v154 offset:0\n\t"
    "v_mfma_f32_16x16x32_bf16 v[28:31], v[120:123], v[140:143], v[28:31]\n\t"
    "ds_read_b64_tr_b16 v[102:103], v154 offset:512\n\t"
    "v_mfma_f32_16x16x32_bf16 v[48:51], v[124:127], v[140:143], v[48:51]\n\t"
    "ds_read_b64_tr_b16 v[104:105], v155 offset:0\n\t"
    "v_mfma_f32_16x16x32_bf16 v[68:71], v[128:131], v[140:143], v[68:71]\n\t"
    "ds_read_b64_tr_b16 v[106:107], v155 offset:512\n\t"
    "v_mfma_f32_16x16x32_bf16 v[72:75], v[128:131], v[144:147], v[72:75]\n\t"
    "ds_read_b64_tr_b16 v[108:109], v156 offset:0\n\t"
    "v_mfma_f32_16x16x32_bf16 v[52:55], v[124:127], v[144:147], v[52:55]\n\t"
    "ds_read_b64_tr_b16 v[110:111], v156 offset:512\n\t"
    "v_mfma_f32_16x16x32_bf16 v[32:35], v[120:123], v[144:147], v[32:35]\n\t"
    "ds_read_b64_tr_b16 v[112:113], v157 offset:0\n\t"
    "v_mfma_f32_16x16x32_bf16 v[12:15], v[116:119], v[144:147], v[12:15]\n\t"
    "ds_read_b64_tr_b16 v[114:115], v157 offset:512\n\t"
    "v_mfma_f32_16x16x32_bf16 v[16:19], v[116:119], v[148:151], v[16:19]\n\t"
    "v_mfma_f32_16x16x32_bf16 v[36:39], v[120:123], v[148:151], v[36:39]\n\t"
    "v_mfma_f32_16x16x32_bf16 v[56:59], v[124:127], v[148:151], v[56:59]\n\t"
    "v_mfma_f32_16x16x32_bf16 v[76:79], v[128:131], v[148:151], v[76:79]\n\t"
    "s_waitcnt lgkmcnt(0)\n\t"
    "v_mfma_f32_16x16x32_bf16 v[0:3], v[80:83], v[96:99], v[0:3]\n\t"
    "v_mfma_f32_16x16x32_bf16 v[20:23], v[84:87], v[96:99], v[20:23]\n\t"
    "s_barrier\n\t"
    "v_mfma_f32_16x16x32_bf16 v[40:43], v[88:91], v[96:99], v[40:43]\n\t"
    "ds_read_b64_tr_b16 v[132:133], v153 offset:20480\n\t"
    "v_mfma_f32_16x16x32_bf16 v[60:63], v[92:95], v[96:99], v[60:63]\n\t"
    "ds_read_b64_tr_b16 v[134:135], v153 offset:20992\n\t"
    "v_mfma_f32_16x16x32_bf16 v[64:67], v[92:95], v[100:103], v[64:67]\n\t"
    "ds_read_b128 v[116:119], v152 offset:8192\n\t"
    "v_mfma_f32_16x16x32_bf16 v[44:47], v[88:91], v[100:103], v[44:47]\n\t"
    "ds_read_b128 v[120:123], v152 offset:9216\n\t"
    "v_mfma_f32_16x16x32_bf16 v[24:27], v[84:87], v[100:103], v[24:27]\n\t"
    "ds_read_b128 v[124:127], v152 offset:10240\n\t"
    "v_mfma_f32_16x16x32_bf16 v[4:7], v[80:83], v[100:103], v[4:7]\n\t"
    "ds_read_b128 v[128:131], v152 offset:11264\n\t"
    "v_mfma_f32_16x16x32_bf16 v[8:11], v[80:83], v[104:107], v[8:11]\n\t"
    "ds_read_b64_tr_b16 v[136:137], v154 offset:20480\n\t"
    "v_mfma_f32_16x16x32_bf16 v[28:31], v[84:87], v[104:107], v[28:31]\n\t"
    "ds_read_b64_tr_b16 v[138:139], v154 offset:20992\n\t"
    "v_mfma_f32_16x16x32_bf16 v[48:51], v[88:91], v[104:107], v[48:51]\n\t"
    "ds_read_b64_tr_b16 v[140:141], v155 offset:20480\n\t"
    "v_mfma_f32_16x16x32_bf16 v[68:71], v[92:95], v[104:107], v[68:71]\n\t"
    "ds_read_b64_tr_b16 v[142:143], v155 offset:20992\n\t"
    "v_mfma_f32_16x16x32_bf16 v[72:75], v[92:95], v[108:111], v[72:75]\n\t"
    "ds_read_b64_tr_b16 v[144:145], v156 offset:20480\n\t"
    "v_mfma_f32_16x16x32_bf16 v[52:55], v[88:91], v[108:111], v[52:55]\n\t"
    "ds_read_b64_tr_b16 v[146:147], v156 offset:20992\n\t"
    "v_mfma_f32_16x16x32_bf16 v[32:35], v[84:87], v[108:111], v[32:35]\n\t"
    "ds_read_b64_tr_b16 v[148:149], v157 offset:20480\n\t"
    "v_mfma_f32_16x16x32_bf16 v[12:15], v[80:83], v[108:111], v[12:15]\n\t"
    "ds_read_b64_tr_b16 v[150:151], v157 offset:20992\n\t"
    "v_mfma_f32_16x16x32_bf16 v[16:19], v[80:83], v[112:115], v[16:19]\n\t"
    "v_mfma_f32_16x16x32_bf16 v[36:39], v[84:87], v[112:115], v[36:39]\n\t"
    "v_mfma_f32_16x16x32_bf16 v[56:59], v[88:91], v[112:115], v[56:59]\n\t"
    "v_mfma_f32_16x16x32_bf16 v[76:79], v[92:95], v[112:115], v[76:79]\n\t"
    "s_waitcnt lgkmcnt(0)\n\t"
    "v_mfma_f32_16x16x32_bf16 v[0:3], v[116:119], v[132:135], v[0:3]\n\t"
    "v_mfma_f32_16x16x32_bf16 v[20:23], v[120:123], v[132:135], v[20:23]\n\t"
    "s_barrier\n\t"
    "v_mfma_f32_16x16x32_bf16 v[40:43], v[124:127], v[132:135], v[40:43]\n\t"
    "ds_read_b64_tr_b16 v[96:97], v153 offset:40960\n\t"
    "v_mfma_f32_16x16x32_bf16 v[60:63], v[128:131], v[132:135], v[60:63]\n\t"
    "ds_read_b64_tr_b16 v[98:99], v153 offset:41472\n\t"
    "v_mfma_f32_16x16x32_bf16 v[64:67], v[128:131], v[136:139], v[64:67]\n\t"
    "ds_read_b128 v[80:83], v152 offset:16384\n\t"
    "v_mfma_f32_16x16x32_bf16 v[44:47], v[124:127], v[136:139], v[44:47]\n\t"
    "ds_read_b128 v[84:87], v152 offset:17408\n\t"
    "v_mfma_f32_16x16x32_bf16 v[24:27], v[120:123], v[136:139], v[24:27]\n\t"
    "ds_read_b128 v[88:91], v152 offset:18432\n\t"
    "v_mfma_f32_16x16x32_bf16 v[4:7], v[116:119], v[136:139], v[4:7]\n\t"
    "ds_read_b128 v[92:95], v152 offset:19456\n\t"
    "v_mfma_f32_16x16x32_bf16 v[8:11], v[116:119], v[140:143], v[8:11]\n\t"
    "ds_read_b64_tr_b16 v[100:101], v154 offset:40960\n\t"
    "v_mfma_f32_16x16x32_bf16 v[28:31], v[120:123], v[140:143], v[28:31]\n\t"
    "ds_read_b64_tr_b16 v[102:103], v154 offset:41472\n\t"
    "v_mfma_f32_16x16x32_bf16 v[48:51], v[124:127], v[140:143], v[48:51]\n\t"
    "ds_read_b64_tr_b16 v[104:105], v155 offset:40960\n\t"
    "v_mfma_f32_16x16x32_bf16 v[68:71], v[128:131], v[140:143], v[68:71]\n\t"
    "ds_read_b64_tr_b16 v[106:107], v155 offset:41472\n\t"
    "v_mfma_f32_16x16x32_bf16 v[72:75], v[128:131], v[144:147], v[72:75]\n\t"
    "ds_read_b64_tr_b16 v[108:109], v156 offset:40960\n\t"
    "v_mfma_f32_16x16x32_bf16 v[52:55], v[124:127], v[144:147], v[52:55]\n\t"
    "ds_read_b64_tr_b16 v[110:111], v156 offset:41472\n\t"
    "v_mfma_f32_16x16x32_bf16 v[32:35], v[120:123], v[144:147], v[32:35]\n\t"
    "ds_read_b64_tr_b16 v[112:113], v157 offset:40960\n\t"
    "v_mfma_f32_16x16x32_bf16 v[12:15], v[116:119], v[144:147], v[12:15]\n\t"
    "ds_read_b64_tr_b16 v[114:115], v157 offset:41472\n\t"
    "v_mfma_f32_16x16x32_bf16 v[16:19], v[116:119], v[148:151], v[16:19]\n\t"
    "v_mfma_f32_16x16x32_bf16 v[36:39], v[120:123], v[148:151], v[36:39]\n\t"
    "v_mfma_f32_16x16x32_bf16 v[56:59], v[124:127], v[148:151], v[56:59]\n\t"
    "v_mfma_f32_16x16x32_bf16 v[76:79], v[128:131], v[148:151], v[76:79]\n\t"
    "s_waitcnt lgkmcnt(0)\n\t"
    "v_mfma_f32_16x16x32_bf16 v[0:3], v[80:83], v[96:99], v[0:3]\n\t"
    "v_mfma_f32_16x16x32_bf16 v[20:23], v[84:87], v[96:99], v[20:23]\n\t"
    "s_barrier\n\t"
    "v_mfma_f32_16x16x32_bf16 v[40:43], v[88:91], v[96:99], v[40:43]\n\t"
    "ds_read_b64_tr_b16 v[132:133], v158 offset:0\n\t"
    "v_mfma_f32_16x16x32_bf16 v[60:63], v[92:95], v[96:99], v[60:63]\n\t"
    "ds_read_b64_tr_b16 v[134:135], v158 offset:512\n\t"
    "v_mfma_f32_16x16x32_bf16 v[64:67], v[92:95], v[100:103], v[64:67]\n\t"
    "ds_read_b128 v[116:119], v152 offset:24576\n\t"
    "v_mfma_f32_16x16x32_bf16 v[44:47], v[88:91], v[100:103], v[44:47]\n\t"
    "ds_read_b128 v[120:123], v152 offset:25600\n\t"
    "v_mfma_f32_16x16x32_bf16 v[24:27], v[84:87], v[100:103], v[24:27]\n\t"
    "ds_read_b128 v[124:127], v152 offset:26624\n\t"
    "v_mfma_f32_16x16x32_bf16 v[4:7], v[80:83], v[100:103], v[4:7]\n\t"
    "ds_read_b128 v[128:131], v152 offset:27648\n\t"
    "v_mfma_f32_16x16x32_bf16 v[8:11], v[80:83], v[104:107], v[8:11]\n\t"
    "ds_read_b64_tr_b16 v[136:137], v159 offset:0\n\t"
    "v_mfma_f32_16x16x32_bf16 v[28:31], v[84:87], v[104:107], v[28:31]\n\t"
    "ds_read_b64_tr_b16 v[138:139], v159 offset:512\n\t"
    "v_mfma_f32_16x16x32_bf16 v[48:51], v[88:91], v[104:107], v[48:51]\n\t"
    "ds_read_b64_tr_b16 v[140:141], v160 offset:0\n\t"
    "v_mfma_f32_16x16x32_bf16 v[68:71], v[92:95], v[104:107], v[68:71]\n\t"
    "ds_read_b64_tr_b16 v[142:143], v160 offset:512\n\t"
    "v_mfma_f32_16x16x32_bf16 v[72:75], v[92:95], v[108:111], v[72:75]\n\t"
    "ds_read_b64_tr_b16 v[144:145], v161 offset:0\n\t"
    "v_mfma_f32_16x16x32_bf16 v[52:55], v[88:91], v[108:111], v[52:55]\n\t"
    "ds_read_b64_tr_b16 v[146:147], v161 offset:512\n\t"
    "v_mfma_f32_16x16x32_bf16 v[32:35], v[84:87], v[108:111], v[32:35]\n\t"
    "ds_read_b64_tr_b16 v[148:149], v162 offset:0\n\t"
    "v_mfma_f32_16x16x32_bf16 v[12:15], v[80:83], v[108:111], v[12:15]\n\t"
    "ds_read_b64_tr_b16 v[150:151], v162 offset:512\n\t"
    "v_mfma_f32_16x16x32_bf16 v[16:19], v[80:83], v[112:115], v[16:19]\n\t"
    "v_mfma_f32_16x16x32_bf16 v[36:39], v[84:87], v[112:115], v[36:39]\n\t"
    "v_mfma_f32_16x16x32_bf16 v[56:59], v[88:91], v[112:115], v[56:59]\n\t"
    "v_mfma_f32_16x16x32_bf16 v[76:79], v[92:95], v[112:115], v[76:79]\n\t"
    "s_waitcnt lgkmcnt(0)\n\t"
    "v_mfma_f32_16x16x32_bf16 v[0:3], v[116:119], v[132:135], v[0:3]\n\t"
    "v_mfma_f32_16x16x32_bf16 v[20:23], v[120:123], v[132:135], v[20:23]\n\t"
    "s_barrier\n\t"
    "v_mfma_f32_16x16x32_bf16 v[40:43], v[124:127], v[132:135], v[40:43]\n\t"
    "ds_read_b64_tr_b16 v[96:97], v158 offset:20480\n\t"
    "v_mfma_f32_16x16x32_bf16 v[60:63], v[128:131], v[132:135], v[60:63]\n\t"
    "ds_read_b64_tr_b16 v[98:99], v158 offset:20992\n\t"
    "v_mfma_f32_16x16x32_bf16 v[64:67], v[128:131], v[136:139], v[64:67]\n\t"
    "ds_read_b128 v[80:83], v152 offset:32768\n\t"
    "v_mfma_f32_16x16x32_bf16 v[44:47], v[124:127], v[136:139], v[44:47]\n\t"
    "ds_read_b128 v[84:87], v152 offset:33792\n\t"
    "v_mfma_f32_16x16x32_bf16 v[24:27], v[120:123], v[136:139], v[24:27]\n\t"
    "ds_read_b128 v[88:91], v152 offset:34816\n\t"
    "v_mfma_f32_16x16x32_bf16 v[4:7], v[116:119], v[136:139], v[4:7]\n\t"
    "ds_read_b128 v[92:95], v152 offset:35840\n\t"
    "v_mfma_f32_16x16x32_bf16 v[8:11], v[116:119], v[140:143], v[8:11]\n\t"
    "ds_read_b64_tr_b16 v[100:101], v159 offset:20480\n\t"
    "v_mfma_f32_16x16x32_bf16 v[28:31], v[120:123], v[140:143], v[28:31]\n\t"
    "ds_read_b64_tr_b16 v[102:103], v159 offset:20992\n\t"
    "v_mfma_f32_16x16x32_bf16 v[48:51], v[124:127], v[140:143], v[48:51]\n\t"
    "ds_read_b64_tr_b16 v[104:105], v160 offset:20480\n\t"
    "v_mfma_f32_16x16x32_bf16 v[68:71], v[128:131], v[140:143], v[68:71]\n\t"
    "ds_read_b64_tr_b16 v[106:107], v160 offset:20992\n\t"
    "v_mfma_f32_16x16x32_bf16 v[72:75], v[128:131], v[144:147], v[72:75]\n\t"
    "ds_read_b64_tr_b16 v[108:109], v161 offset:20480\n\t"
    "v_mfma_f32_16x16x32_bf16 v[52:55], v[124:127], v[144:147], v[52:55]\n\t"
    "ds_read_b64_tr_b16 v[110:111], v161 offset:20992\n\t"
    "v_mfma_f32_16x16x32_bf16 v[32:35], v[120:123], v[144:147], v[32:35]\n\t"
    "ds_read_b64_tr_b16 v[112:113], v162 offset:20480\n\t"
    "v_mfma_f32_16x16x32_bf16 v[12:15], v[116:119], v[144:147], v[12:15]\n\t"
    "ds_read_b64_tr_b16 v[114:115], v162 offset:20992\n\t"
    "v_mfma_f32_16x16x32_bf16 v[16:19], v[116:119], v[148:151], v[16:19]\n\t"
    "v_mfma_f32_16x16x32_bf16 v[36:39], v[120:123], v[148:151], v[36:39]\n\t"
    "v_mfma_f32_16x16x32_bf16 v[56:59], v[124:127], v[148:151], v[56:59]\n\t"
    "v_mfma_f32_16x16x32_bf16 v[76:79], v[128:131], v[148:151], v[76:79]\n\t"
    "s_waitcnt lgkmcnt(0)\n\t"
    "v_mfma_f32_16x16x32_bf16 v[0:3], v[80:83], v[96:99], v[0:3]\n\t"
    "v_mfma_f32_16x16x32_bf16 v[20:23], v[84:87], v[96:99], v[20:23]\n\t"
    "s_barrier\n\t"
    "v_mfma_f32_16x16x32_bf16 v[40:43], v[88:91], v[96:99], v[40:43]\n\t"
    "ds_read_b64_tr_b16 v[132:133], v153 offset:0\n\t"
    "v_mfma_f32_16x16x32_bf16 v[60:63], v[92:95], v[96:99], v[60:63]\n\t"
    "ds_read_b64_tr_b16 v[134:135], v153 offset:512\n\t"
    "v_mfma_f32_16x16x32_bf16 v[64:67], v[92:95], v[100:103], v[64:67]\n\t"
    "ds_read_b128 v[116:119], v152 offset:0\n\t"
    "v_mfma_f32_16x16x32_bf16 v[44:47], v[88:91], v[100:103], v[44:47]\n\t"
    "ds_read_b128 v[120:123], v152 offset:1024\n\t"
    "v_mfma_f32_16x16x32_bf16 v[24:27], v[84:87], v[100:103], v[24:27]\n\t"
    "ds_read_b128 v[124:127], v152 offset:2048\n\t"
    "v_mfma_f32_16x16x32_bf16 v[4:7], v[80:83], v[100:103], v[4:7]\n\t"
    "ds_read_b128 v[128:131], v152 offset:3072\n\t"
    "v_mfma_f32_16x16x32_bf16 v[8:11], v[80:83], v[104:107], v[8:11]\n\t"
    "ds_read_b64_tr_b16 v[136:137], v154 offset:0\n\t"
    "v_mfma_f32_16x16x32_bf16 v[28:31], v[84:87], v[104:107], v[28:31]\n\t"
    "ds_read_b64_tr_b16 v[138:139], v154 offset:512\n\t"
    "v_mfma_f32_16x16x32_bf16 v[48:51], v[88:91], v[104:107], v[48:51]\n\t"
    "ds_read_b64_tr_b16 v[140:141], v155 offset:0\n\t"
    "v_mfma_f32_16x16x32_bf16 v[68:71], v[92:95], v[104:107], v[68:71]\n\t"
    "ds_read_b64_tr_b16 v[142:143], v155 offset:512\n\t"
    "v_mfma_f32_16x16x32_bf16 v[72:75], v[92:95], v[108:111], v[72:75]\n\t"
    "ds_read_b64_tr_b16 v[144:145], v156 offset:0\n\t"
    "v_mfma_f32_16x16x32_bf16 v[52:55], v[88:91], v[108:111], v[52:55]\n\t"
    "ds_read_b64_tr_b16 v[146:147], v156 offset:512\n\t"
    "v_mfma_f32_16x16x32_bf16 v[32:35], v[84:87], v[108:111], v[32:35]\n\t"
    "ds_read_b64_tr_b16 v[148:149], v157 offset:0\n\t"
    "v_mfma_f32_16x16x32_bf16 v[12:15], v[80:83], v[108:111], v[12:15]\n\t"
    "ds_read_b64_tr_b16 v[150:151], v157 offset:512\n\t"
    "v_mfma_f32_16x16x32_bf16 v[16:19], v[80:83], v[112:115], v[16:19]\n\t"
    "v_mfma_f32_16x16x32_bf16 v[36:39], v[84:87], v[112:115], v[36:39]\n\t"
    "v_mfma_f32_16x16x32_bf16 v[56:59], v[88:91], v[112:115], v[56:59]\n\t"
    "v_mfma_f32_16x16x32_bf16 v[76:79], v[92:95], v[112:115], v[76:79]\n\t"
    "s_waitcnt lgkmcnt(0)\n\t"
    "v_mfma_f32_16x16x32_bf16 v[0:3], v[116:119], v[132:135], v[0:3]\n\t"
    "v_mfma_f32_16x16x32_bf16 v[20:23], v[120:123], v[132:135], v[20:23]\n\t"
    "s_barrier\n\t"
    "v_mfma_f32_16x16x32_bf16 v[40:43], v[124:127], v[132:135], v[40:43]\n\t"
    "ds_read_b64_tr_b16 v[96:97], v153 offset:20480\n\t"
    "v_mfma_f32_16x16x32_bf16 v[60:63], v[128:131], v[132:135], v[60:63]\n\t"
    "ds_read_b64_tr_b16 v[98:99], v153 offset:20992\n\t"
    "v_mfma_f32_16x16x32_bf16 v[64:67], v[128:131], v[136:139], v[64:67]\n\t"
    "ds_read_b128 v[80:83], v152 offset:8192\n\t"
    "v_mfma_f32_16x16x32_bf16 v[44:47], v[124:127], v[136:139], v[44:47]\n\t"
    "ds_read_b128 v[84:87], v152 offset:9216\n\t"
    "v_mfma_f32_16x16x32_bf16 v[24:27], v[120:123], v[136:139], v[24:27]\n\t"
    "ds_read_b128 v[88:91], v152 offset:10240\n\t"
    "v_mfma_f32_16x16x32_bf16 v[4:7], v[116:119], v[136:139], v[4:7]\n\t"
    "ds_read_b128 v[92:95], v152 offset:11264\n\t"
    "v_mfma_f32_16x16x32_bf16 v[8:11], v[116:119], v[140:143], v[8:11]\n\t"
    "ds_read_b64_tr_b16 v[100:101], v154 offset:20480\n\t"
    "v_mfma_f32_16x16x32_bf16 v[28:31], v[120:123], v[140:143], v[28:31]\n\t"
    "ds_read_b64_tr_b16 v[102:103], v154 offset:20992\n\t"
    "v_mfma_f32_16x16x32_bf16 v[48:51], v[124:127], v[140:143], v[48:51]\n\t"
    "ds_read_b64_tr_b16 v[104:105], v155 offset:20480\n\t"
    "v_mfma_f32_16x16x32_bf16 v[68:71], v[128:131], v[140:143], v[68:71]\n\t"
    "ds_read_b64_tr_b16 v[106:107], v155 offset:20992\n\t"
    "v_mfma_f32_16x16x32_bf16 v[72:75], v[128:131], v[144:147], v[72:75]\n\t"
    "ds_read_b64_tr_b16 v[108:109], v156 offset:20480\n\t"
    "v_mfma_f32_16x16x32_bf16 v[52:55], v[124:127], v[144:147], v[52:55]\n\t"
    "ds_read_b64_tr_b16 v[110:111], v156 offset:20992\n\t"
    "v_mfma_f32_16x16x32_bf16 v[32:35], v[120:123], v[144:147], v[32:35]\n\t"
    "ds_read_b64_tr_b16 v[112:113], v157 offset:20480\n\t"
    "v_mfma_f32_16x16x32_bf16 v[12:15], v[116:119], v[144:147], v[12:15]\n\t"
    "ds_read_b64_tr_b16 v[114:115], v157 offset:20992\n\t"
    "v_mfma_f32_16x16x32_bf16 v[16:19], v[116:119], v[148:151], v[16:19]\n\t"
    "v_mfma_f32_16x16x32_bf16 v[36:39], v[120:123], v[148:151], v[36:39]\n\t"
    "v_mfma_f32_16x16x32_bf16 v[56:59], v[124:127], v[148:151], v[56:59]\n\t"
    "v_mfma_f32_16x16x32_bf16 v[76:79], v[128:131], v[148:151], v[76:79]\n\t"
    "s_waitcnt lgkmcnt(0)\n\t"
    "v_mfma_f32_16x16x32_bf16 v[0:3], v[80:83], v[96:99], v[0:3]\n\t"
    "v_mfma_f32_16x16x32_bf16 v[20:23], v[84:87], v[96:99], v[20:23]\n\t"
    "s_barrier\n\t"
    "s_setprio 1\n\t"
    "s_cmp_gt_u32 %[wv], 3\n\t"
    "s_cbranch_scc1 Lpxp_%=\n\t"
    "s_setprio 2\n\t"
    "Lpxp_%=:\n\t"
    "v_mfma_f32_16x16x32_bf16 v[40:43], v[88:91], v[96:99], v[40:43]\n\t"
    "ds_read_b64_tr_b16 v[132:133], v153 offset:40960\n\t"
    "v_mfma_f32_16x16x32_bf16 v[60:63], v[92:95], v[96:99], v[60:63]\n\t"
    "ds_read_b64_tr_b16 v[134:135], v153 offset:41472\n\t"
    "v_mfma_f32_16x16x32_bf16 v[64:67], v[92:95], v[100:103], v[64:67]\n\t"
    "ds_read_b128 v[116:119], v152 offset:16384\n\t"
    "v_mfma_f32_16x16x32_bf16 v[44:47], v[88:91], v[100:103], v[44:47]\n\t"
    "ds_read_b128 v[120:123], v152 offset:17408\n\t"
    "v_mfma_f32_16x16x32_bf16 v[24:27], v[84:87], v[100:103], v[24:27]\n\t"
    "ds_read_b128 v[124:127], v152 offset:18432\n\t"
    "v_mfma_f32_16x16x32_bf16 v[4:7], v[80:83], v[100:103], v[4:7]\n\t"
    "ds_read_b128 v[128:131], v152 offset:19456\n\t"
    "v_mfma_f32_16x16x32_bf16 v[8:11], v[80:83], v[104:107], v[8:11]\n\t"
    "ds_read_b64_tr_b16 v[136:137], v154 offset:40960\n\t"
    "v_mfma_f32_16x16x32_bf16 v[28:31], v[84:87], v[104:107], v[28:31]\n\t"
    "ds_read_b64_tr_b16 v[138:139], v154 offset:41472\n\t"
    "v_mfma_f32_16x16x32_bf16 v[48:51], v[88:91], v[104:107], v[48:51]\n\t"
    "ds_read_b64_tr_b16 v[140:141], v155 offset:40960\n\t"
    "v_mfma_f32_16x16x32_bf16 v[68:71], v[92:95], v[104:107], v[68:71]\n\t"
    "ds_read_b64_tr_b16 v[142:143], v155 offset:41472\n\t"
    "v_mfma_f32_16x16x32_bf16 v[72:75], v[92:95], v[108:111], v[72:75]\n\t"
    "ds_read_b64_tr_b16 v[144:145], v156 offset:40960\n\t"
    "v_mfma_f32_16x16x32_bf16 v[52:55], v[88:91], v[108:111], v[52:55]\n\t"
    "ds_read_b64_tr_b16 v[146:147], v156 offset:41472\n\t"
    "v_mfma_f32_16x16x32_bf16 v[32:35], v[84:87], v[108:111], v[32:35]\n\t"
    "ds_read_b64_tr_b16 v[148:149], v157 offset:40960\n\t"
    "v_mfma_f32_16x16x32_bf16 v[12:15], v[80:83], v[108:111], v[12:15]\n\t"
    "ds_read_b64_tr_b16 v[150:151], v157 offset:41472\n\t"
    "v_mfma_f32_16x16x32_bf16 v[16:19], v[80:83], v[112:115], v[16:19]\n\t"
    "v_mfma_f32_16x16x32_bf16 v[36:39], v[84:87], v[112:115], v[36:39]\n\t"
    "v_mfma_f32_16x16x32_bf16 v[56:59], v[88:91], v[112:115], v[56:59]\n\t"
    "v_mfma_f32_16x16x32_bf16 v[76:79], v[92:95], v[112:115], v[76:79]\n\t"
    "s_waitcnt lgkmcnt(0)\n\t"
    "v_mfma_f32_16x16x32_bf16 v[0:3], v[116:119], v[132:135], v[0:3]\n\t"
    "v_mfma_f32_16x16x32_bf16 v[20:23], v[120:123], v[132:135], v[20:23]\n\t"
    "v_mfma_f32_16x16x32_bf16 v[40:43], v[124:127], v[132:135], v[40:43]\n\t"
    "ds_read_b64_tr_b16 v[96:97], v158 offset:0\n\t"
    "v_mfma_f32_16x16x32_bf16 v[60:63], v[128:131], v[132:135], v[60:63]\n\t"
    "ds_read_b64_tr_b16 v[98:99], v158 offset:512\n\t"
    "v_mfma_f32_16x16x32_bf16 v[64:67], v[128:131], v[136:139], v[64:67]\n\t"
    "ds_read_b128 v[80:83], v152 offset:24576\n\t"
    "v_mfma_f32_16x16x32_bf16 v[44:47], v[124:127], v[136:139], v[44:47]\n\t"
    "ds_read_b128 v[84:87], v152 offset:25600\n\t"
    "v_mfma_f32_16x16x32_bf16 v[24:27], v[120:123], v[136:139], v[24:27]\n\t"
    "ds_read_b128 v[88:91], v152 offset:26624\n\t"
    "v_mfma_f32_16x16x32_bf16 v[4:7], v[116:119], v[136:139], v[4:7]\n\t"
    "ds_read_b128 v[92:95], v152 offset:27648\n\t"
    "v_mfma_f32_16x16x32_bf16 v[8:11], v[116:119], v[140:143], v[8:11]\n\t"
    "ds_read_b64_tr_b16 v[100:101], v159 offset:0\n\t"
    "v_mfma_f32_16x16x32_bf16 v[28:31], v[120:123], v[140:143], v[28:31]\n\t"
    "ds_read_b64_tr_b16 v[102:103], v159 offset:512\n\t"
    "v_mfma_f32_16x16x32_bf16 v[48:51], v[124:127], v[140:143], v[48:51]\n\t"
    "ds_read_b64_tr_b16 v[104:105], v160 offset:0\n\t"
    "v_mfma_f32_16x16x32_bf16 v[68:71], v[128:131], v[140:143], v[68:71]\n\t"
    "ds_read_b64_tr_b16 v[106:107], v160 offset:512\n\t"
    "v_mfma_f32_16x16x32_bf16 v[72:75], v[128:131], v[144:147], v[72:75]\n\t"
    "ds_read_b64_tr_b16 v[108:109], v161 offset:0\n\t"
    "v_mfma_f32_16x16x32_bf16 v[52:55], v[124:127], v[144:147], v[52:55]\n\t"
    "ds_read_b64_tr_b16 v[110:111], v161 offset:512\n\t"
    "v_mfma_f32_16x16x32_bf16 v[32:35], v[120:123], v[144:147], v[32:35]\n\t"
    "ds_read_b64_tr_b16 v[112:113], v162 offset:0\n\t"
    "v_mfma_f32_16x16x32_bf16 v[12:15], v[116:119], v[144:147], v[12:15]\n\t"
    "ds_read_b64_tr_b16 v[114:115], v162 offset:512\n\t"
    "v_mfma_f32_16x16x32_bf16 v[16:19], v[116:119], v[148:151], v[16:19]\n\t"
    "v_mfma_f32_16x16x32_bf16 v[36:39], v[120:123], v[148:151], v[36:39]\n\t"
    "v_mfma_f32_16x16x32_bf16 v[56:59], v[124:127], v[148:151], v[56:59]\n\t"
    "v_mfma_f32_16x16x32_bf16 v[76:79], v[128:131], v[148:151], v[76:79]\n\t"
    "s_waitcnt lgkmcnt(0)\n\t"
    "v_mfma_f32_16x16x32_bf16 v[0:3], v[80:83], v[96:99], v[0:3]\n\t"
    "v_mfma_f32_16x16x32_bf16 v[20:23], v[84:87], v[96:99], v[20:23]\n\t"
    "v_mfma_f32_16x16x32_bf16 v[40:43], v[88:91], v[96:99], v[40:43]\n\t"
    "ds_read_b64_tr_b16 v[132:133], v158 offset:20480\n\t"
    "v_mfma_f32_16x16x32_bf16 v[60:63], v[92:95], v[96:99], v[60:63]\n\t"
    "ds_read_b64_tr_b16 v[134:135], v158 offset:20992\n\t"
    "v_mfma_f32_16x16x32_bf16 v[64:67], v[92:95], v[100:103], v[64:67]\n\t"
    "ds_read_b128 v[116:119], v152 offset:32768\n\t"
    "v_mfma_f32_16x16x32_bf16 v[44:47], v[88:91], v[100:103], v[44:47]\n\t"
    "ds_read_b128 v[120:123], v152 offset:33792\n\t"
    "v_mfma_f32_16x16x32_bf16 v[24:27], v[84:87], v[100:103], v[24:27]\n\t"
    "ds_read_b128 v[124:127], v152 offset:34816\n\t"
    "v_mfma_f32_16x16x32_bf16 v[4:7], v[80:83], v[100:103], v[4:7]\n\t"
    "ds_read_b128 v[128:131], v152 offset:35840\n\t"
    "v_mfma_f32_16x16x32_bf16 v[8:11], v[80:83], v[104:107], v[8:11]\n\t"
    "ds_read_b64_tr_b16 v[136:137], v159 offset:20480\n\t"
    "v_mfma_f32_16x16x32_bf16 v[28:31], v[84:87], v[104:107], v[28:31]\n\t"
    "ds_read_b64_tr_b16 v[138:139], v159 offset:20992\n\t"
    "v_mfma_f32_16x16x32_bf16 v[48:51], v[88:91], v[104:107], v[48:51]\n\t"
    "ds_read_b64_tr_b16 v[140:141], v160 offset:20480\n\t"
    "v_mfma_f32_16x16x32_bf16 v[68:71], v[92:95], v[104:107], v[68:71]\n\t"
    "ds_read_b64_tr_b16 v[142:143], v160 offset:20992\n\t"
    "v_mfma_f32_16x16x32_bf16 v[72:75], v[92:95], v[108:111], v[72:75]\n\t"
    "ds_read_b64_tr_b16 v[144:145], v161 offset:20480\n\t"
    "v_mfma_f32_16x16x32_bf16 v[52:55], v[88:91], v[108:111], v[52:55]\n\t"
    "ds_read_b64_tr_b16 v[146:147], v161 offset:20992\n\t"
    "v_mfma_f32_16x16x32_bf16 v[32:35], v[84:87], v[108:111], v[32:35]\n\t"
    "ds_read_b64_tr_b16 v[148:149], v162 offset:20480\n\t"
    "v_mfma_f32_16x16x32_bf16 v[12:15], v[80:83], v[108:111], v[12:15]\n\t"
    "ds_read_b64_tr_b16 v[150:151], v162 offset:20992\n\t"
    "v_mfma_f32_16x16x32_bf16 v[16:19], v[80:83], v[112:115], v[16:19]\n\t"
    "v_mfma_f32_16x16x32_bf16 v[36:39], v[84:87], v[112:115], v[36:39]\n\t"
    "v_mfma_f32_16x16x32_bf16 v[56:59], v[88:91], v[112:115], v[56:59]\n\t"
    "v_mfma_f32_16x16x32_bf16 v[76:79], v[92:95], v[112:115], v[76:79]\n\t"
    "s_waitcnt lgkmcnt(0)\n\t"
    "s_mov_b64 exec, 1\n\t"
    "ds_add_u32 v164, v165\n\t"
    "s_mov_b64 exec, -1\n\t"
    "v_mfma_f32_16x16x32_bf16 v[0:3], v[116:119], v[132:135], v[0:3]\n\t"
    "v_mbcnt_lo_u32_b32 v152, -1, 0\n\t"
    "v_mbcnt_hi_u32_b32 v152, -1, v152\n\t"
    "v_mfma_f32_16x16x32_bf16 v[20:23], v[120:123], v[132:135], v[20:23]\n\t"
    "v_and_b32 v153, 15, v152\n\t"
    "v_lshrrev_b32 v154, 5, v152\n\t"
    "v_mfma_f32_16x16x32_bf16 v[40:43], v[124:127], v[132:135], v[40:43]\n\t"
    "v_mul_u32_u24 v154, 640, v154\n\t"
    "v_bfe_u32 v152, v152, 4, 1\n\t"
    "v_mfma_f32_16x16x32_bf16 v[60:63], v[128:131], v[132:135], v[60:63]\n\t"
    "v_mad_u32_u24 v154, v152, %[c320], v154\n\t"
    "v_mfma_f32_16x16x32_bf16 v[4:7], v[116:119], v[136:139], v[4:7]\n\t"
    "v_mfma_f32_16x16x32_bf16 v[24:27], v[120:123], v[136:139], v[24:27]\n\t"
    "v_mfma_f32_16x16x32_bf16 v[44:47], v[124:127], v[136:139], v[44:47]\n\t"
    "v_mfma_f32_16x16x32_bf16 v[64:67], v[128:131], v[136:139], v[64:67]\n\t"
    "v_mfma_f32_16x16x32_bf16 v[8:11], v[116:119], v[140:143], v[8:11]\n\t"
    "v_add_u32 v152, 0, v153\n\t"
    "v_mul_u32_u24 v161, 205, v152\n\t"
    "v_lshrrev_b32 v161, 12, v161\n\t"
    "v_lshl_add_u32 v152, v152, 4, v154\n\t"
    "v_mfma_f32_16x16x32_bf16 v[28:31], v[120:123], v[140:143], v[28:31]\n\t"
    "v_mad_u32_u24 v155, v161, %[chd], v152\n\t"
    "v_cvt_pk_bf16_f32 v80, v0, v1\n\t"
    "v_cvt_pk_bf16_f32 v81, v2, v3\n\t"
    "v_cvt_pk_bf16_f32 v82, v20, v21\n\t"
    "v_mfma_f32_16x16x32_bf16 v[48:51], v[124:127], v[140:143], v[48:51]\n\t"
    "v_cvt_pk_bf16_f32 v83, v22, v23\n\t"
    "buffer_store_dwordx4 v[80:83], v155, %[rs], %[ob] offen offset:0 sc1\n\t"
    "v_cvt_pk_bf16_f32 v84, v40, v41\n\t"
    "v_cvt_pk_bf16_f32 v85, v42, v43\n\t"
    "v_mfma_f32_16x16x32_bf16 v[68:71], v[128:131], v[140:143], v[68:71]\n\t"
    "v_cvt_pk_bf16_f32 v86, v60, v61\n\t"
    "v_cvt_pk_bf16_f32 v87, v62, v63\n\t"
    "buffer_store_dwordx4 v[84:87], v155, %[rs], %[ob] offen offset:1280 sc1\n\t"
    "v_mfma_f32_16x16x32_bf16 v[12:15], v[116:119], v[144:147], v[12:15]\n\t"
    "v_add_u32 v152, 16, v153\n\t"
    "v_mul_u32_u24 v161, 205, v152\n\t"
    "v_lshrrev_b32 v161, 12, v161\n\t"
    "v_lshl_add_u32 v152, v152, 4, v154\n\t"
    "v_mfma_f32_16x16x32_bf16 v[32:35], v[120:123], v[144:147], v[32:35]\n\t"
    "v_mad_u32_u24 v156, v161, %[chd], v152\n\t"
    "v_cvt_pk_bf16_f32 v88, v4, v5\n\t"
    "v_cvt_pk_bf16_f32 v89, v6, v7\n\t"
    "v_cvt_pk_bf16_f32 v90, v24, v25\n\t"
    "v_mfma_f32_16x16x32_bf16 v[52:55], v[124:127], v[144:147], v[52:55]\n\t"
    "v_cvt_pk_bf16_f32 v91, v26, v27\n\t"
    "buffer_store_dwordx4 v[88:91], v156, %[rs], %[ob] offen offset:0 sc1\n\t"
    "v_cvt_pk_bf16_f32 v92, v44, v45\n\t"
    "v_cvt_pk_bf16_f32 v93, v46, v47\n\t"
    "v_mfma_f32_16x16x32_bf16 v[72:75], v[128:131], v[144:147], v[72:75]\n\t"
    "v_cvt_pk_bf16_f32 v94, v64, v65\n\t"
    "v_cvt_pk_bf16_f32 v95, v66, v67\n\t"
    "buffer_store_dwordx4 v[92:95], v156, %[rs], %[ob] offen offset:1280 sc1\n\t"
    "v_mfma_f32_16x16x32_bf16 v[16:19], v[116:119], v[148:151], v[16:19]\n\t"
    "v_add_u32 v152, 32, v153\n\t"
    "v_mul_u32_u24 v161, 205, v152\n\t"
    "v_lshrrev_b32 v161, 12, v161\n\t"
    "v_lshl_add_u32 v152, v152, 4, v154\n\t"
    "v_mfma_f32_16x16x32_bf16 v[36:39], v[120:123], v[148:151], v[36:39]\n\t"
    "v_mad_u32_u24 v157, v161, %[chd], v152\n\t"
    "v_cvt_pk_bf16_f32 v96, v8, v9\n\t"
    "v_cvt_pk_bf16_f32 v97, v10, v11\n\t"
    "v_cvt_pk_bf16_f32 v98, v28, v29\n\t"
    "v_mfma_f32_16x16x32_bf16 v[56:59], v[124:127], v[148:151], v[56:59]\n\t"
    "v_cvt_pk_bf16_f32 v99, v30, v31\n\t"
    "buffer_store_dwordx4 v[96:99], v157, %[rs], %[ob] offen offset:0 sc1\n\t"
    "v_cvt_pk_bf16_f32 v100, v48, v49\n\t"
    "v_cvt_pk_bf16_f32 v101, v50, v51\n\t"
    "v_mfma_f32_16x16x32_bf16 v[76:79], v[128:131], v[148:151], v[76:79]\n\t"
    "v_cvt_pk_bf16_f32 v102, v68, v69\n\t"
    "v_cvt_pk_bf16_f32 v103, v70, v71\n\t"
    "buffer_store_dwordx4 v[100:103], v157, %[rs], %[ob] offen offset:1280 sc1\n\t"
    "s_nop 3\n\t"
    "v_add_u32 v152, 48, v153\n\t"
    "v_mul_u32_u24 v161, 205, v152\n\t"
    "v_lshrrev_b32 v161, 12, v161\n\t"
    "v_lshl_add_u32 v152, v152, 4, v154\n\t"
    "v_mad_u32_u24 v158, v161, %[chd], v152\n\t"
    "v_cvt_pk_bf16_f32 v104, v12, v13\n\t"
    "v_cvt_pk_bf16_f32 v105, v14, v15\n\t"
    "v_cvt_pk_bf16_f32 v106, v32, v33\n\t"
    "v_cvt_pk_bf16_f32 v107, v34, v35\n\t"
    "buffer_store_dwordx4 v[104:107], v158, %[rs], %[ob] offen offset:0 sc1\n\t"
    "v_cvt_pk_bf16_f32 v108, v52, v53\n\t"
    "v_cvt_pk_bf16_f32 v109, v54, v55\n\t"
    "v_cvt_pk_bf16_f32 v110, v72, v73\n\t"
    "v_cvt_pk_bf16_f32 v111, v74, v75\n\t"
    "buffer_store_dwordx4 v[108:111], v158, %[rs], %[ob] offen offset:1280 sc1\n\t"
    "v_add_u32 v152, 64, v153\n\t"
    "v_mul_u32_u24 v161, 205, v152\n\t"
    "v_lshrrev_b32 v161, 12, v161\n\t"
    "v_lshl_add_u32 v152, v152, 4, v154\n\t"
    "v_mad_u32_u24 v159, v161, %[chd], v152\n\t"
    "v_cvt_pk_bf16_f32 v160, v16, v17\n\t"
    "v_cvt_pk_bf16_f32 v161, v18, v19\n\t"
    "v_cvt_pk_bf16_f32 v162, v36, v37\n\t"
    "v_cvt_pk_bf16_f32 v163, v38, v39\n\t"
    "buffer_store_dwordx4 v[160:163], v159, %[rs], %[ob] offen offset:0 sc1\n\t"
    "v_cvt_pk_bf16_f32 v112, v56, v57\n\t"
    "v_cvt_pk_bf16_f32 v113, v58, v59\n\t"
    "v_cvt_pk_bf16_f32 v114, v76, v77\n\t"
    "v_cvt_pk_bf16_f32 v115, v78, v79\n\t"
    "buffer_store_dwordx4 v[112:115], v159, %[rs], %[ob] offen offset:1280 sc1\n\t"
    "Lend_%=:\n\t"
    : "+{v[152:159]}"(ha), [so] "=&s"(so)
    : "{v164}"(LDS_CNT), "{v165}"(1), [wv] "s"(wv), [xa] "s"(xa), [rs] "s"(rs), [ob] "s"(ob), [o2] "s"(o2), [sc] "s"(sc), [nl] "s"(nl), [c320] "s"(320), [chd] "s"(32 * 5120 - 320)
    : "v0", "v1", "v2", "v3", "v4", "v5", "v6", "v7", "v8", "v9", "v10", "v11", "v12", "v13", "v14", "v15", "v16", "v17", "v18", "v19", "v20", "v21", "v22", "v23", "v24", "v25", "v26", "v27", "v28", "v29", "v30", "v31", "v32", "v33", "v34", "v35", "v36", "v37", "v38", "v39", "v40", "v41", "v42", "v43", "v44", "v45", "v46", "v47", "v48", "v49", "v50", "v51", "v52", "v53", "v54", "v55", "v56", "v57", "v58", "v59", "v60", "v61", "v62", "v63", "v64", "v65", "v66", "v67", "v68", "v69", "v70", "v71", "v72", "v73", "v74", "v75", "v76", "v77", "v78", "v79", "v80", "v81", "v82", "v83", "v84", "v85", "v86", "v87", "v88", "v89", "v90", "v91", "v92", "v93", "v94", "v95", "v96", "v97", "v98", "v99", "v100", "v101", "v102", "v103", "v104", "v105", "v106", "v107", "v108", "v109", "v110", "v111", "v112", "v113", "v114", "v115", "v116", "v117", "v118", "v119", "v120", "v121", "v122", "v123", "v124", "v125", "v126", "v127", "v128", "v129", "v130", "v131", "v132", "v133", "v134", "v135", "v136", "v137", "v138", "v139", "v140", "v141", "v142", "v143", "v144", "v145", "v146", "v147", "v148", "v149", "v150", "v151", "v160", "v161", "v162", "v163", "scc", "memory");
}

__device__ __forceinline__ void k1_pre_qkv(u64 xb, u64 wb, u64 w3b, unsigned vx0, unsigned vw, unsigned vw3, u64 vs, unsigned lw, unsigned stride) {
  unsigned cnt; const u64 st = stride;
  wb += 0 * st; w3b += 0 * st; vs += 0 * st;
  asm volatile(
    "s_nop 4\n\t"
    "s_add_u32 m0, %[mw], 0\n\t"
    "s_nop 0\n\t"
    "global_load_lds_dwordx4 %[vw], s[42:43] offset:0\n\t"
    "s_add_u32 m0, %[mw], 3968\n\t"
    "s_nop 0\n\t"
    "global_load_lds_dwordx4 %[vw], s[42:43] offset:128\n\t"
    "s_add_u32 m0, %[mw], 7936\n\t"
    "s_nop 0\n\t"
    "global_load_lds_dwordx4 %[vw], s[42:43] offset:256\n\t"
    "s_add_u32 m0, %[mw], 11904\n\t"
    "s_nop 0\n\t"
    "global_load_lds_dwordx4 %[vw], s[42:43] offset:384\n\t"
    "s_add_u32 m0, %[mw], 15872\n\t"
    "s_nop 0\n\t"
    "global_load_lds_dwordx4 %[vw], s[42:43] offset:512\n\t"
    "s_add_u32 s42, s42, %[st32]\n\t"
    "s_addc_u32 s43, s43, 0\n\t"
    "s_add_u32 m0, %[mw], 20480\n\t"
    "s_nop 0\n\t"
    "global_load_lds_dwordx4 %[vw], s[42:43] offset:0\n\t"
    "s_add_u32 m0, %[mw], 24448\n\t"
    "s_nop 0\n\t"
    "global_load_lds_dwordx4 %[vw], s[42:43] offset:128\n\t"
    "s_add_u32 m0, %[mw], 28416\n\t"
    "s_nop 0\n\t"
    "global_load_lds_dwordx4 %[vw], s[42:43] offset:256\n\t"
    "s_add_u32 m0, %[mw], 32384\n\t"
    "s_nop 0\n\t"
    "global_load_lds_dwordx4 %[vw], s[42:43] offset:384\n\t"
    "s_add_u32 m0, %[mw], 36352\n\t"
    "s_nop 0\n\t"
    "global_load_lds_dwordx4 %[vw], s[42:43] offset:512\n\t"
    "s_add_u32 s42, s42, %[st32]\n\t"
    "s_addc_u32 s43, s43, 0\n\t"
    "s_add_u32 m0, %[mw], 40960\n\t"
    "s_nop 0\n\t"
    "global_load_lds_dwordx4 %[vw], s[42:43] offset:0\n\t"
    "s_add_u32 m0, %[mw], 44928\n\t"
    "s_nop 0\n\t"
    "global_load_lds_dwordx4 %[vw], s[42:43] offset:128\n\t"
    "s_add_u32 m0, %[mw], 48896\n\t"
    "s_nop 0\n\t"
    "global_load_lds_dwordx4 %[vw], s[42:43] offset:256\n\t"
    "s_add_u32 m0, %[mw], 52864\n\t"
    "s_nop 0\n\t"
    "global_load_lds_dwordx4 %[vw], s[42:43] offset:384\n\t"
    "s_add_u32 m0, %[mw], 56832\n\t"
    "s_nop 0\n\t"
    "global_load_lds_dwordx4 %[vw], s[42:43] offset:512\n\t"
    "s_add_u32 s42, s42, %[st32]\n\t"
    "s_addc_u32 s43, s43, 0\n\t"
    "s_add_u32 m0, %[mw], 61440\n\t"
    "s_nop 0\n\t"
    "global_load_lds_dwordx4 %[vw], s[42:43] offset:0\n\t"
    "s_add_u32 m0, %[mw], 65408\n\t"
    "s_nop 0\n\t"
    "global_load_lds_dwordx4 %[vw], s[42:43] offset:128\n\t"
    "s_add_u32 m0, %[mw], 69376\n\t"
    "s_nop 0\n\t"
    "global_load_lds_dwordx4 %[vw], s[42:43] offset:256\n\t"
    "s_add_u32 m0, %[mw], 73344\n\t"
    "s_nop 0\n\t"
    "global_load_lds_dwordx4 %[vw], s[42:43] offset:384\n\t"
    "s_add_u32 m0, %[mw], 77312\n\t"
    "s_nop 0\n\t"
    "global_load_lds_dwordx4 %[vw], s[42:43] offset:512\n\t"
    "s_add_u32 s42, s42, %[st32]\n\t"
    "s_addc_u32 s43, s43, 0\n\t"
    "s_add_u32 m0, %[mw], 81920\n\t"
    "s_nop 0\n\t"
    "global_load_lds_dwordx4 %[vw], s[42:43] offset:0\n\t"
    "s_add_u32 m0, %[mw], 85888\n\t"
    "s_nop 0\n\t"
    "global_load_lds_dwordx4 %[vw], s[42:43] offset:128\n\t"
    "s_add_u32 m0, %[mw], 89856\n\t"
    "s_nop 0\n\t"
    "global_load_lds_dwordx4 %[vw], s[42:43] offset:256\n\t"
    "s_add_u32 m0, %[mw], 93824\n\t"
    "s_nop 0\n\t"
    "global_load_lds_dwordx4 %[vw], s[42:43] offset:384\n\t"
    "s_add_u32 m0, %[mw], 97792\n\t"
    "s_nop 0\n\t"
    "global_load_lds_dwordx4 %[vw], s[42:43] offset:512\n\t"
    "s_add_u32 s42, s42, %[st32]\n\t"
    "s_addc_u32 s43, s43, 0\n\t"
    : "+&{s[40:41]}"(xb), "+&{s[42:43]}"(wb), "+&{s[44:45]}"(w3b), [vs] "+&v"(vs), [cnt] "=&s"(cnt)
    : [vx0] "v"(vx0), [vx1] "v"(vx0 + 4 * 20480), [vw] "v"(vw), [vw3] "v"(vw3), [mx] "s"(lw * 1024), [mw] "s"(40960 + lw * 1024), [st] "s"(st), [st32] "s"(stride), [ca] "v"(LDS_CNT + (vx0 & 1023) / 4), [cz] "v"(0)
    : "scc", "memory");
}

__device__ __forceinline__ void k1_load_qkv(u64 xb, u64 wb, u64 w3b, unsigned vx0, unsigned vw, unsigned vw3, u64 vs, unsigned lw, unsigned stride) {
  unsigned cnt; const u64 st = stride;
  wb += 5 * st; w3b += 5 * st; vs += 5 * st;
  asm volatile(
    "s_nop 4\n\t"
    "s_add_u32 m0, %[mx], 0\n\t"
    "s_nop 0\n\t"
    "global_load_lds_dwordx4 %[vx0], s[40:41] sc1\n\t"
    "s_add_u32 m0, %[mx], 4096\n\t"
    "s_nop 0\n\t"
    "global_load_lds_dwordx4 %[vx1], s[40:41] sc1\n\t"
    "s_add_u32 s40, s40, 0x400\n\t"
    "s_addc_u32 s41, s41, 0\n\t"
    "s_add_u32 m0, %[mx], 8192\n\t"
    "s_nop 0\n\t"
    "global_load_lds_dwordx4 %[vx0], s[40:41] sc1\n\t"
    "s_add_u32 m0, %[mx], 12288\n\t"
    "s_nop 0\n\t"
    "global_load_lds_dwordx4 %[vx1], s[40:41] sc1\n\t"
    "s_add_u32 s40, s40, 0x400\n\t"
    "s_addc_u32 s41, s41, 0\n\t"
    "s_add_u32 m0, %[mx], 16384\n\t"
    "s_nop 0\n\t"
    "global_load_lds_dwordx4 %[vx0], s[40:41] sc1\n\t"
    "s_add_u32 m0, %[mx], 20480\n\t"
    "s_nop 0\n\t"
    "global_load_lds_dwordx4 %[vx1], s[40:41] sc1\n\t"
    "s_add_u32 s40, s40, 0x400\n\t"
    "s_addc_u32 s41, s41, 0\n\t"
    "s_add_u32 m0, %[mx], 24576\n\t"
    "s_nop 0\n\t"
    "global_load_lds_dwordx4 %[vx0], s[40:41] sc1\n\t"
    "s_add_u32 m0, %[mx], 28672\n\t"
    "s_nop 0\n\t"
    "global_load_lds_dwordx4 %[vx1], s[40:41] sc1\n\t"
    "s_add_u32 s40, s40, 0x400\n\t"
    "s_addc_u32 s41, s41, 0\n\t"
    "s_add_u32 m0, %[mx], 32768\n\t"
    "s_nop 0\n\t"
    "global_load_lds_dwordx4 %[vx0], s[40:41] sc1\n\t"
    "s_add_u32 m0, %[mx], 36864\n\t"
    "s_nop 0\n\t"
    "global_load_lds_dwordx4 %[vx1], s[40:41] sc1\n\t"
    "s_add_u32 s40, s40, 0x400\n\t"
    "s_addc_u32 s41, s41, 0\n\t"
    "s_waitcnt vmcnt(8)\n\t"
    "s_barrier\n\t"
    "s_mov_b64 exec, 0xffff\n\t"
    "ds_write_b32 %[ca], %[cz]\n\t"
    "s_mov_b64 exec, -1\n\t"
    "s_waitcnt lgkmcnt(0)\n\t"
    "s_waitcnt vmcnt(6)\n\t"
    "s_barrier\n\t"
    "s_add_u32 m0, %[mx], 0\n\t"
    "s_nop 0\n\t"
    "global_load_lds_dwordx4 %[vx0], s[40:41] sc1\n\t"
    "s_add_u32 m0, %[mx], 4096\n\t"
    "s_nop 0\n\t"
    "global_load_lds_dwordx4 %[vx1], s[40:41] sc1\n\t"
    "s_add_u32 s40, s40, 0x400\n\t"
    "s_addc_u32 s41, s41, 0\n\t"
    "s_add_u32 m0, %[mw], 0\n\t"
    "s_nop 0\n\t"
    "global_load_lds_dwordx4 %[vw], s[42:43] offset:0\n\t"
    "s_add_u32 m0, %[mw], 3968\n\t"
    "s_nop 0\n\t"
    "global_load_lds_dwordx4 %[vw], s[42:43] offset:128\n\t"
    "s_add_u32 m0, %[mw], 7936\n\t"
    "s_nop 0\n\t"
    "global_load_lds_dwordx4 %[vw], s[42:43] offset:256\n\t"
    "s_add_u32 m0, %[mw], 11904\n\t"
    "s_nop 0\n\t"
    "global_load_lds_dwordx4 %[vw], s[42:43] offset:384\n\t"
    "s_add_u32 m0, %[mw], 15872\n\t"
    "s_nop 0\n\t"
    "global_load_lds_dwordx4 %[vw], s[42:43] offset:512\n\t"
    "s_add_u32 s42, s42, %[st32]\n\t"
    "s_addc_u32 s43, s43, 0\n\t"
    "s_waitcnt vmcnt(11)\n\t"
    "s_barrier\n\t"
    "s_add_u32 m0, %[mx], 8192\n\t"
    "s_nop 0\n\t"
    "global_load_lds_dwordx4 %[vx0], s[40:41] sc1\n\t"
    "s_add_u32 m0, %[mx], 12288\n\t"
    "s_nop 0\n\t"
    "global_load_lds_dwordx4 %[vx1], s[40:41] sc1\n\t"
    "s_add_u32 s40, s40, 0x400\n\t"
    "s_addc_u32 s41, s41, 0\n\t"
    "s_add_u32 m0, %[mw], 20480\n\t"
    "s_nop 0\n\t"
    "global_load_lds_dwordx4 %[vw], s[42:43] offset:0\n\t"
    "s_add_u32 m0, %[mw], 24448\n\t"
    "s_nop 0\n\t"
    "global_load_lds_dwordx4 %[vw], s[42:43] offset:128\n\t"
    "s_add_u32 m0, %[mw], 28416\n\t"
    "s_nop 0\n\t"
    "global_load_lds_dwordx4 %[vw], s[42:43] offset:256\n\t"
    "s_add_u32 m0, %[mw], 32384\n\t"
    "s_nop 0\n\t"
    "global_load_lds_dwordx4 %[vw], s[42:43] offset:384\n\t"
    "s_add_u32 m0, %[mw], 36352\n\t"
    "s_nop 0\n\t"
    "global_load_lds_dwordx4 %[vw], s[42:43] offset:512\n\t"
    "s_add_u32 s42, s42, %[st32]\n\t"
    "s_addc_u32 s43, s43, 0\n\t"
    "s_waitcnt vmcnt(16)\n\t"
    "s_barrier\n\t"
    "s_add_u32 m0, %[mx], 16384\n\t"
    "s_nop 0\n\t"
    "global_load_lds_dwordx4 %[vx0], s[40:41] sc1\n\t"
    "s_add_u32 m0, %[mx], 20480\n\t"
    "s_nop 0\n\t"
    "global_load_lds_dwordx4 %[vx1], s[40:41] sc1\n\t"
    "s_add_u32 s40, s40, 0x400\n\t"
    "s_addc_u32 s41, s41, 0\n\t"
    "s_add_u32 m0, %[mw], 40960\n\t"
    "s_nop 0\n\t"
    "global_load_lds_dwordx4 %[vw], s[42:43] offset:0\n\t"
    "s_add_u32 m0, %[mw], 44928\n\t"
    "s_nop 0\n\t"
    "global_load_lds_dwordx4 %[vw], s[42:43] offset:128\n\t"
    "s_add_u32 m0, %[mw], 48896\n\t"
    "s_nop 0\n\t"
    "global_load_lds_dwordx4 %[vw], s[42:43] offset:256\n\t"
    "s_add_u32 m0, %[mw], 52864\n\t"
    "s_nop 0\n\t"
    "global_load_lds_dwordx4 %[vw], s[42:43] offset:384\n\t"
    "s_add_u32 m0, %[mw], 56832\n\t"
    "s_nop 0\n\t"
    "global_load_lds_dwordx4 %[vw], s[42:43] offset:512\n\t"
    "s_add_u32 s42, s42, %[st32]\n\t"
    "s_addc_u32 s43, s43, 0\n\t"
    "s_waitcnt vmcnt(21)\n\t"
    "s_barrier\n\t"
    "s_add_u32 m0, %[mx], 24576\n\t"
    "s_nop 0\n\t"
    "global_load_lds_dwordx4 %[vx0], s[40:41] sc1\n\t"
    "s_add_u32 m0, %[mx], 28672\n\t"
    "s_nop 0\n\t"
    "global_load_lds_dwordx4 %[vx1], s[40:41] sc1\n\t"
    "s_add_u32 s40, s40, 0x400\n\t"
    "s_addc_u32 s41, s41, 0\n\t"
    "s_add_u32 m0, %[mw], 61440\n\t"
    "s_nop 0\n\t"
    "global_load_lds_dwordx4 %[vw], s[42:43] offset:0\n\t"
    "s_add_u32 m0, %[mw], 65408\n\t"
    "s_nop 0\n\t"
    "global_load_lds_dwordx4 %[vw], s[42:43] offset:128\n\t"
    "s_add_u32 m0, %[mw], 69376\n\t"
    "s_nop 0\n\t"
    "global_load_lds_dwordx4 %[vw], s[42:43] offset:256\n\t"
    "s_add_u32 m0, %[mw], 73344\n\t"
    "s_nop 0\n\t"
    "global_load_lds_dwordx4 %[vw], s[42:43] offset:384\n\t"
    "s_add_u32 m0, %[mw], 77312\n\t"
    "s_nop 0\n\t"
    "global_load_lds_dwordx4 %[vw], s[42:43] offset:512\n\t"
    "s_add_u32 s42, s42, %[st32]\n\t"
    "s_addc_u32 s43, s43, 0\n\t"
    "s_waitcnt vmcnt(21)\n\t"
    "s_barrier\n\t"
    "s_add_u32 m0, %[mx], 32768\n\t"
    "s_nop 0\n\t"
    "global_load_lds_dwordx4 %[vx0], s[40:41] sc1\n\t"
    "s_add_u32 m0, %[mx], 36864\n\t"
    "s_nop 0\n\t"
    "global_load_lds_dwordx4 %[vx1], s[40:41] sc1\n\t"
    "s_add_u32 s40, s40, 0x400\n\t"
    "s_addc_u32 s41, s41, 0\n\t"
    "s_add_u32 m0, %[mw], 81920\n\t"
    "s_nop 0\n\t"
    "global_load_lds_dwordx4 %[vw], s[42:43] offset:0\n\t"
    "s_add_u32 m0, %[mw], 85888\n\t"
    "s_nop 0\n\t"
    "global_load_lds_dwordx4 %[vw], s[42:43] offset:128\n\t"
    "s_add_u32 m0, %[mw], 89856\n\t"
    "s_nop 0\n\t"
    "global_load_lds_dwordx4 %[vw], s[42:43] offset:256\n\t"
    "s_add_u32 m0, %[mw], 93824\n\t"
    "s_nop 0\n\t"
    "global_load_lds_dwordx4 %[vw], s[42:43] offset:384\n\t"
    "s_add_u32 m0, %[mw], 97792\n\t"
    "s_nop 0\n\t"
    "global_load_lds_dwordx4 %[vw], s[42:43] offset:512\n\t"
    "s_add_u32 s42, s42, %[st32]\n\t"
    "s_addc_u32 s43, s43, 0\n\t"
    "s_mov_b32 %[cnt], 2\n\t"
    "Lloop_%=:\n\t"
    "s_waitcnt vmcnt(21)\n\t"
    "s_barrier\n\t"
    "s_add_u32 m0, %[mx], 0\n\t"
    "s_nop 0\n\t"
    "global_load_lds_dwordx4 %[vx0], s[40:41] sc1\n\t"
    "s_add_u32 m0, %[mx], 4096\n\t"
    "s_nop 0\n\t"
    "global_load_lds_dwordx4 %[vx1], s[40:41] sc1\n\t"
    "s_add_u32 s40, s40, 0x400\n\t"
    "s_addc_u32 s41, s41, 0\n\t"
    "s_add_u32 m0, %[mw], 0\n\t"
    "s_nop 0\n\t"
    "global_load_lds_dwordx4 %[vw], s[42:43] offset:0\n\t"
    "s_add_u32 m0, %[mw], 3968\n\t"
    "s_nop 0\n\t"
    "global_load_lds_dwordx4 %[vw], s[42:43] offset:128\n\t"
    "s_add_u32 m0, %[mw], 7936\n\t"
    "s_nop 0\n\t"
    "global_load_lds_dwordx4 %[vw], s[42:43] offset:256\n\t"
    "s_add_u32 m0, %[mw], 11904\n\t"
    "s_nop 0\n\t"
    "global_load_lds_dwordx4 %[vw], s[42:43] offset:384\n\t"
    "s_add_u32 m0, %[mw], 15872\n\t"
    "s_nop 0\n\t"
    "global_load_lds_dwordx4 %[vw], s[42:43] offset:512\n\t"
    "s_add_u32 s42, s42, %[st32]\n\t"
    "s_addc_u32 s43, s43, 0\n\t"
    "s_waitcnt vmcnt(21)\n\t"
    "s_barrier\n\t"
    "s_add_u32 m0, %[mx], 8192\n\t"
    "s_nop 0\n\t"
    "global_load_lds_dwordx4 %[vx0], s[40:41] sc1\n\t"
    "s_add_u32 m0, %[mx], 12288\n\t"
    "s_nop 0\n\t"
    "global_load_lds_dwordx4 %[vx1], s[40:41] sc1\n\t"
    "s_add_u32 s40, s40, 0x400\n\t"
    "s_addc_u32 s41, s41, 0\n\t"
    "s_add_u32 m0, %[mw], 20480\n\t"
    "s_nop 0\n\t"
    "global_load_lds_dwordx4 %[vw], s[42:43] offset:0\n\t"
    "s_add_u32 m0, %[mw], 24448\n\t"
    "s_nop 0\n\t"
    "global_load_lds_dwordx4 %[vw], s[42:43] offset:128\n\t"
    "s_add_u32 m0, %[mw], 28416\n\t"
    "s_nop 0\n\t"
    "global_load_lds_dwordx4 %[vw], s[42:43] offset:256\n\t"
    "s_add_u32 m0, %[mw], 32384\n\t"
    "s_nop 0\n\t"
    "global_load_lds_dwordx4 %[vw], s[42:43] offset:384\n\t"
    "s_add_u32 m0, %[mw], 36352\n\t"
    "s_nop 0\n\t"
    "global_load_lds_dwordx4 %[vw], s[42:43] offset:512\n\t"
    "s_add_u32 s42, s42, %[st32]\n\t"
    "s_addc_u32 s43, s43, 0\n\t"
    "s_waitcnt vmcnt(21)\n\t"
    "s_barrier\n\t"
    "s_add_u32 m0, %[mx], 16384\n\t"
    "s_nop 0\n\t"
    "global_load_lds_dwordx4 %[vx0], s[40:41] sc1\n\t"
    "s_add_u32 m0, %[mx], 20480\n\t"
    "s_nop 0\n\t"
    "global_load_lds_dwordx4 %[vx1], s[40:41] sc1\n\t"
    "s_add_u32 s40, s40, 0x400\n\t"
    "s_addc_u32 s41, s41, 0\n\t"
    "s_add_u32 m0, %[mw], 40960\n\t"
    "s_nop 0\n\t"
    "global_load_lds_dwordx4 %[vw], s[42:43] offset:0\n\t"
    "s_add_u32 m0, %[mw], 44928\n\t"
    "s_nop 0\n\t"
    "global_load_lds_dwordx4 %[vw], s[42:43] offset:128\n\t"
    "s_add_u32 m0, %[mw], 48896\n\t"
    "s_nop 0\n\t"
    "global_load_lds_dwordx4 %[vw], s[42:43] offset:256\n\t"
    "s_add_u32 m0, %[mw], 52864\n\t"
    "s_nop 0\n\t"
    "global_load_lds_dwordx4 %[vw], s[42:43] offset:384\n\t"
    "s_add_u32 m0, %[mw], 56832\n\t"
    "s_nop 0\n\t"
    "global_load_lds_dwordx4 %[vw], s[42:43] offset:512\n\t"
    "s_add_u32 s42, s42, %[st32]\n\t"
    "s_addc_u32 s43, s43, 0\n\t"
    "s_waitcnt vmcnt(21)\n\t"
    "s_barrier\n\t"
    "s_add_u32 m0, %[mx], 24576\n\t"
    "s_nop 0\n\t"
    "global_load_lds_dwordx4 %[vx0], s[40:41] sc1\n\t"
    "s_add_u32 m0, %[mx], 28672\n\t"
    "s_nop 0\n\t"
    "global_load_lds_dwordx4 %[vx1], s[40:41] sc1\n\t"
    "s_add_u32 s40, s40, 0x400\n\t"
    "s_addc_u32 s41, s41, 0\n\t"
    "s_add_u32 m0, %[mw], 61440\n\t"
    "s_nop 0\n\t"
    "global_load_lds_dwordx4 %[vw], s[42:43] offset:0\n\t"
    "s_add_u32 m0, %[mw], 65408\n\t"
    "s_nop 0\n\t"
    "global_load_lds_dwordx4 %[vw], s[42:43] offset:128\n\t"
    "s_add_u32 m0, %[mw], 69376\n\t"
    "s_nop 0\n\t"
    "global_load_lds_dwordx4 %[vw], s[42:43] offset:256\n\t"
    "s_add_u32 m0, %[mw], 73344\n\t"
    "s_nop 0\n\t"
    "global_load_lds_dwordx4 %[vw], s[42:43] offset:384\n\t"
    "s_add_u32 m0, %[mw], 77312\n\t"
    "s_nop 0\n\t"
    "global_load_lds_dwordx4 %[vw], s[42:43] offset:512\n\t"
    "s_add_u32 s42, s42, %[st32]\n\t"
    "s_addc_u32 s43, s43, 0\n\t"
    "s_waitcnt vmcnt(21)\n\t"
    "s_barrier\n\t"
    "s_add_u32 m0, %[mx], 32768\n\t"
    "s_nop 0\n\t"
    "global_load_lds_dwordx4 %[vx0], s[40:41] sc1\n\t"
    "s_add_u32 m0, %[mx], 36864\n\t"
    "s_nop 0\n\t"
    "global_load_lds_dwordx4 %[vx1], s[40:41] sc1\n\t"
    "s_add_u32 s40, s40, 0x400\n\t"
    "s_addc_u32 s41, s41, 0\n\t"
    "s_add_u32 m0, %[mw], 81920\n\t"
    "s_nop 0\n\t"
    "global_load_lds_dwordx4 %[vw], s[42:43] offset:0\n\t"
    "s_add_u32 m0, %[mw], 85888\n\t"
    "s_nop 0\n\t"
    "global_load_lds_dwordx4 %[vw], s[42:43] offset:128\n\t"
    "s_add_u32 m0, %[mw], 89856\n\t"
    "s_nop 0\n\t"
    "global_load_lds_dwordx4 %[vw], s[42:43] offset:256\n\t"
    "s_add_u32 m0, %[mw], 93824\n\t"
    "s_nop 0\n\t"
    "global_load_lds_dwordx4 %[vw], s[42:43] offset:384\n\t"
    "s_add_u32 m0, %[mw], 97792\n\t"
    "s_nop 0\n\t"
    "global_load_lds_dwordx4 %[vw], s[42:43] offset:512\n\t"
    "s_add_u32 s42, s42, %[st32]\n\t"
    "s_addc_u32 s43, s43, 0\n\t"
    "s_sub_u32 %[cnt], %[cnt], 1\n\t"
    "s_cmp_lg_u32 %[cnt], 0\n\t"
    "s_cbranch_scc1 Lloop_%=\n\t"
    "s_waitcnt vmcnt(21)\n\t"
    "s_barrier\n\t"
    "s_waitcnt vmcnt(0)\n\t"
    "s_barrier\n\t"
    : "+&{s[40:41]}"(xb), "+&{s[42:43]}"(wb), "+&{s[44:45]}"(w3b), [vs] "+&v"(vs), [cnt] "=&s"(cnt)
    : [vx0] "v"(vx0), [vx1] "v"(vx0 + 4 * 20480), [vw] "v"(vw), [vw3] "v"(vw3), [mx] "s"(lw * 1024), [mw] "s"(40960 + lw * 1024), [st] "s"(st), [st32] "s"(stride), [ca] "v"(LDS_CNT + (vx0 & 1023) / 4), [cz] "v"(0)
    : "scc", "memory");
}

__device__ __forceinline__ void k1_pre_ffn(u64 xb, u64 wb, u64 w3b, unsigned vx0, unsigned vw, unsigned vw3, u64 vs, unsigned lw, unsigned stride) {
  unsigned cnt; const u64 st = stride;
  wb += 0 * st; w3b += 0 * st; vs += 0 * st;
  asm volatile(
    "s_nop 4\n\t"
    "s_add_u32 m0, %[mw], 0\n\t"
    "s_nop 0\n\t"
    "global_load_lds_dwordx4 %[vw], s[42:43] offset:0\n\t"
    "s_add_u32 m0, %[mw], 3968\n\t"
    "s_nop 0\n\t"
    "global_load_lds_dwordx4 %[vw], s[42:43] offset:128\n\t"
    "s_add_u32 m0, %[mw], 8192\n\t"
    "s_nop 0\n\t"
    "global_load_lds_dwordx4 %[vs], off\n\t"
    "v_lshl_add_u64 %[vs], %[st], 0, %[vs]\n\t"
    "s_add_u32 m0, %[mw], 12288\n\t"
    "s_nop 0\n\t"
    "global_load_lds_dwordx4 %[vw3], s[44:45] offset:0\n\t"
    "s_add_u32 m0, %[mw], 16256\n\t"
    "s_nop 0\n\t"
    "global_load_lds_dwordx4 %[vw3], s[44:45] offset:128\n\t"
    "s_add_u32 s42, s42, %[st32]\n\t"
    "s_addc_u32 s43, s43, 0\n\t"
    "s_add_u32 s44, s44, %[st32]\n\t"
    "s_addc_u32 s45, s45, 0\n\t"
    "s_add_u32 m0, %[mw], 20480\n\t"
    "s_nop 0\n\t"
    "global_load_lds_dwordx4 %[vw], s[42:43] offset:0\n\t"
    "s_add_u32 m0, %[mw], 24448\n\t"
    "s_nop 0\n\t"
    "global_load_lds_dwordx4 %[vw], s[42:43] offset:128\n\t"
    "s_add_u32 m0, %[mw], 28672\n\t"
    "s_nop 0\n\t"
    "global_load_lds_dwordx4 %[vs], off\n\t"
    "v_lshl_add_u64 %[vs], %[st], 0, %[vs]\n\t"
    "s_add_u32 m0, %[mw], 32768\n\t"
    "s_nop 0\n\t"
    "global_load_lds_dwordx4 %[vw3], s[44:45] offset:0\n\t"
    "s_add_u32 m0, %[mw], 36736\n\t"
    "s_nop 0\n\t"
    "global_load_lds_dwordx4 %[vw3], s[44:45] offset:128\n\t"
    "s_add_u32 s42, s42, %[st32]\n\t"
    "s_addc_u32 s43, s43, 0\n\t"
    "s_add_u32 s44, s44, %[st32]\n\t"
    "s_addc_u32 s45, s45, 0\n\t"
    "s_add_u32 m0, %[mw], 40960\n\t"
    "s_nop 0\n\t"
    "global_load_lds_dwordx4 %[vw], s[42:43] offset:0\n\t"
    "s_add_u32 m0, %[mw], 44928\n\t"
    "s_nop 0\n\t"
    "global_load_lds_dwordx4 %[vw], s[42:43] offset:128\n\t"
    "s_add_u32 m0, %[mw], 49152\n\t"
    "s_nop 0\n\t"
    "global_load_lds_dwordx4 %[vs], off\n\t"
    "v_lshl_add_u64 %[vs], %[st], 0, %[vs]\n\t"
    "s_add_u32 m0, %[mw], 53248\n\t"
    "s_nop 0\n\t"
    "global_load_lds_dwordx4 %[vw3], s[44:45] offset:0\n\t"
    "s_add_u32 m0, %[mw], 57216\n\t"
    "s_nop 0\n\t"
    "global_load_lds_dwordx4 %[vw3], s[44:45] offset:128\n\t"
    "s_add_u32 s42, s42, %[st32]\n\t"
    "s_addc_u32 s43, s43, 0\n\t"
    "s_add_u32 s44, s44, %[st32]\n\t"
    "s_addc_u32 s45, s45, 0\n\t"
    "s_add_u32 m0, %[mw], 61440\n\t"
    "s_nop 0\n\t"
    "global_load_lds_dwordx4 %[vw], s[42:43] offset:0\n\t"
    "s_add_u32 m0, %[mw], 65408\n\t"
    "s_nop 0\n\t"
    "global_load_lds_dwordx4 %[vw], s[42:43] offset:128\n\t"
    "s_add_u32 m0, %[mw], 69632\n\t"
    "s_nop 0\n\t"
    "global_load_lds_dwordx4 %[vs], off\n\t"
    "v_lshl_add_u64 %[vs], %[st], 0, %[vs]\n\t"
    "s_add_u32 m0, %[mw], 73728\n\t"
    "s_nop 0\n\t"
    "global_load_lds_dwordx4 %[vw3], s[44:45] offset:0\n\t"
    "s_add_u32 m0, %[mw], 77696\n\t"
    "s_nop 0\n\t"
    "global_load_lds_dwordx4 %[vw3], s[44:45] offset:128\n\t"
    "s_add_u32 s42, s42, %[st32]\n\t"
    "s_addc_u32 s43, s43, 0\n\t"
    "s_add_u32 s44, s44, %[st32]\n\t"
    "s_addc_u32 s45, s45, 0\n\t"
    "s_add_u32 m0, %[mw], 81920\n\t"
    "s_nop 0\n\t"
    "global_load_lds_dwordx4 %[vw], s[42:43] offset:0\n\t"
    "s_add_u32 m0, %[mw], 85888\n\t"
    "s_nop 0\n\t"
    "global_load_lds_dwordx4 %[vw], s[42:43] offset:128\n\t"
    "s_add_u32 m0, %[mw], 90112\n\t"
    "s_nop 0\n\t"
    "global_load_lds_dwordx4 %[vs], off\n\t"
    "v_lshl_add_u64 %[vs], %[st], 0, %[vs]\n\t"
    "s_add_u32 m0, %[mw], 94208\n\t"
    "s_nop 0\n\t"
    "global_load_lds_dwordx4 %[vw3], s[44:45] offset:0\n\t"
    "s_add_u32 m0, %[mw], 98176\n\t"
    "s_nop 0\n\t"
    "global_load_lds_dwordx4 %[vw3], s[44:45] offset:128\n\t"
    "s_add_u32 s42, s42, %[st32]\n\t"
    "s_addc_u32 s43, s43, 0\n\t"
    "s_add_u32 s44, s44, %[st32]\n\t"
    "s_addc_u32 s45, s45, 0\n\t"
    : "+&{s[40:41]}"(xb), "+&{s[42:43]}"(wb), "+&{s[44:45]}"(w3b), [vs] "+&v"(vs), [cnt] "=&s"(cnt)
    : [vx0] "v"(vx0), [vx1] "v"(vx0 + 4 * 20480), [vw] "v"(vw), [vw3] "v"(vw3), [mx] "s"(lw * 1024), [mw] "s"(40960 + lw * 1024), [st] "s"(st), [st32] "s"(stride), [ca] "v"(LDS_CNT + (vx0 & 1023) / 4), [cz] "v"(0)
    : "scc", "memory");
}

__device__ __forceinline__ void k1_load_ffn(u64 xb, u64 wb, u64 w3b, unsigned vx0, unsigned vw, unsigned vw3, u64 vs, unsigned lw, unsigned stride, u32x4 (&rt)[5], u64 rb, const unsigned (&ro)[5]) {
  unsigned cnt; const u64 st = stride;
  wb += 5 * st; w3b += 5 * st; vs += 5 * st;
  asm volatile(
    "s_nop 4\n\t"
    "s_add_u32 m0, %[mx], 0\n\t"
    "s_nop 0\n\t"
    "global_load_lds_dwordx4 %[vx0], s[40:41] sc1\n\t"
    "s_add_u32 m0, %[mx], 4096\n\t"
    "s_nop 0\n\t"
    "global_load_lds_dwordx4 %[vx1], s[40:41] sc1\n\t"
    "s_add_u32 s40, s40, 0x400\n\t"
    "s_addc_u32 s41, s41, 0\n\t"
    "s_add_u32 m0, %[mx], 8192\n\t"
    "s_nop 0\n\t"
    "global_load_lds_dwordx4 %[vx0], s[40:41] sc1\n\t"
    "s_add_u32 m0, %[mx], 12288\n\t"
    "s_nop 0\n\t"
    "global_load_lds_dwordx4 %[vx1], s[40:41] sc1\n\t"
    "s_add_u32 s40, s40, 0x400\n\t"
    "s_addc_u32 s41, s41, 0\n\t"
    "s_add_u32 m0, %[mx], 16384\n\t"
    "s_nop 0\n\t"
    "global_load_lds_dwordx4 %[vx0], s[40:41] sc1\n\t"
    "s_add_u32 m0, %[mx], 20480\n\t"
    "s_nop 0\n\t"
    "global_load_lds_dwordx4 %[vx1], s[40:41] sc1\n\t"
    "s_add_u32 s40, s40, 0x400\n\t"
    "s_addc_u32 s41, s41, 0\n\t"
    "s_add_u32 m0, %[mx], 24576\n\t"
    "s_nop 0\n\t"
    "global_load_lds_dwordx4 %[vx0], s[40:41] sc1\n\t"
    "s_add_u32 m0, %[mx], 28672\n\t"
    "s_nop 0\n\t"
    "global_load_lds_dwordx4 %[vx1], s[40:41] sc1\n\t"
    "s_add_u32 s40, s40, 0x400\n\t"
    "s_addc_u32 s41, s41, 0\n\t"
    "s_add_u32 m0, %[mx], 32768\n\t"
    "s_nop 0\n\t"
    "global_load_lds_dwordx4 %[vx0], s[40:41] sc1\n\t"
    "s_add_u32 m0, %[mx], 36864\n\t"
    "s_nop 0\n\t"
    "global_load_lds_dwordx4 %[vx1], s[40:41] sc1\n\t"
    "s_add_u32 s40, s40, 0x400\n\t"
    "s_addc_u32 s41, s41, 0\n\t"
    "s_waitcnt vmcnt(8)\n\t"
    "s_barrier\n\t"
    "s_mov_b64 exec, 0xffff\n\t"
    "ds_write_b32 %[ca], %[cz]\n\t"
    "s_mov_b64 exec, -1\n\t"
    "s_waitcnt lgkmcnt(0)\n\t"
    "s_waitcnt vmcnt(6)\n\t"
    "s_barrier\n\t"
    "s_add_u32 m0, %[mx], 0\n\t"
    "s_nop 0\n\t"
    "global_load_lds_dwordx4 %[vx0], s[40:41] sc1\n\t"
    "s_add_u32 m0, %[mx], 4096\n\t"
    "s_nop 0\n\t"
    "global_load_lds_dwordx4 %[vx1], s[40:41] sc1\n\t"
    "s_add_u32 s40, s40, 0x400\n\t"
    "s_addc_u32 s41, s41, 0\n\t"
    "s_add_u32 m0, %[mw], 0\n\t"
    "s_nop 0\n\t"
    "global_load_lds_dwordx4 %[vw], s[42:43] offset:0\n\t"
    "s_add_u32 m0, %[mw], 3968\n\t"
    "s_nop 0\n\t"
    "global_load_lds_dwordx4 %[vw], s[42:43] offset:128\n\t"
    "s_add_u32 m0, %[mw], 8192\n\t"
    "s_nop 0\n\t"
    "global_load_lds_dwordx4 %[vs], off\n\t"
    "v_lshl_add_u64 %[vs], %[st], 0, %[vs]\n\t"
    "s_add_u32 m0, %[mw], 12288\n\t"
    "s_nop 0\n\t"
    "global_load_lds_dwordx4 %[vw3], s[44:45] offset:0\n\t"
    "s_add_u32 m0, %[mw], 16256\n\t"
    "s_nop 0\n\t"
    "global_load_lds_dwordx4 %[vw3], s[44:45] offset:128\n\t"
    "s_add_u32 s42, s42, %[st32]\n\t"
    "s_addc_u32 s43, s43, 0\n\t"
    "s_add_u32 s44, s44, %[st32]\n\t"
    "s_addc_u32 s45, s45, 0\n\t"
    "s_waitcnt vmcnt(11)\n\t"
    "s_barrier\n\t"
    "s_add_u32 m0, %[mx], 8192\n\t"
    "s_nop 0\n\t"
    "global_load_lds_dwordx4 %[vx0], s[40:41] sc1\n\t"
    "s_add_u32 m0, %[mx], 12288\n\t"
    "s_nop 0\n\t"
    "global_load_lds_dwordx4 %[vx1], s[40:41] sc1\n\t"
    "s_add_u32 s40, s40, 0x400\n\t"
    "s_addc_u32 s41, s41, 0\n\t"
    "s_add_u32 m0, %[mw], 20480\n\t"
    "s_nop 0\n\t"
    "global_load_lds_dwordx4 %[vw], s[42:43] offset:0\n\t"
    "s_add_u32 m0, %[mw], 24448\n\t"
    "s_nop 0\n\t"
    "global_load_lds_dwordx4 %[vw], s[42:43] offset:128\n\t"
    "s_add_u32 m0, %[mw], 28672\n\t"
    "s_nop 0\n\t"
    "global_load_lds_dwordx4 %[vs], off\n\t"
    "v_lshl_add_u64 %[vs], %[st], 0, %[vs]\n\t"
    "s_add_u32 m0, %[mw], 32768\n\t"
    "s_nop 0\n\t"
    "global_load_lds_dwordx4 %[vw3], s[44:45] offset:0\n\t"
    "s_add_u32 m0, %[mw], 36736\n\t"
    "s_nop 0\n\t"
    "global_load_lds_dwordx4 %[vw3], s[44:45] offset:128\n\t"
    "s_add_u32 s42, s42, %[st32]\n\t"
    "s_addc_u32 s43, s43, 0\n\t"
    "s_add_u32 s44, s44, %[st32]\n\t"
    "s_addc_u32 s45, s45, 0\n\t"
    "s_waitcnt vmcnt(16)\n\t"
    "s_barrier\n\t"
    "s_add_u32 m0, %[mx], 16384\n\t"
    "s_nop 0\n\t"
    "global_load_lds_dwordx4 %[vx0], s[40:41] sc1\n\t"
    "s_add_u32 m0, %[mx], 20480\n\t"
    "s_nop 0\n\t"
    "global_load_lds_dwordx4 %[vx1], s[40:41] sc1\n\t"
    "s_add_u32 s40, s40, 0x400\n\t"
    "s_addc_u32 s41, s41, 0\n\t"
    "s_add_u32 m0, %[mw], 40960\n\t"
    "s_nop 0\n\t"
    "global_load_lds_dwordx4 %[vw], s[42:43] offset:0\n\t"
    "s_add_u32 m0, %[mw], 44928\n\t"
    "s_nop 0\n\t"
    "global_load_lds_dwordx4 %[vw], s[42:43] offset:128\n\t"
    "s_add_u32 m0, %[mw], 49152\n\t"
    "s_nop 0\n\t"
    "global_load_lds_dwordx4 %[vs], off\n\t"
    "v_lshl_add_u64 %[vs], %[st], 0, %[vs]\n\t"
    "s_add_u32 m0, %[mw], 53248\n\t"
    "s_nop 0\n\t"
    "global_load_lds_dwordx4 %[vw3], s[44:45] offset:0\n\t"
    "s_add_u32 m0, %[mw], 57216\n\t"
    "s_nop 0\n\t"
    "global_load_lds_dwordx4 %[vw3], s[44:45] offset:128\n\t"
    "s_add_u32 s42, s42, %[st32]\n\t"
    "s_addc_u32 s43, s43, 0\n\t"
    "s_add_u32 s44, s44, %[st32]\n\t"
    "s_addc_u32 s45, s45, 0\n\t"
    "s_waitcnt vmcnt(21)\n\t"
    "s_barrier\n\t"
    "s_add_u32 m0, %[mx], 24576\n\t"
    "s_nop 0\n\t"
    "global_load_lds_dwordx4 %[vx0], s[40:41] sc1\n\t"
    "s_add_u32 m0, %[mx], 28672\n\t"
    "s_nop 0\n\t"
    "global_load_lds_dwordx4 %[vx1], s[40:41] sc1\n\t"
    "s_add_u32 s40, s40, 0x400\n\t"
    "s_addc_u32 s41, s41, 0\n\t"
    "s_add_u32 m0, %[mw], 61440\n\t"
    "s_nop 0\n\t"
    "global_load_lds_dwordx4 %[vw], s[42:43] offset:0\n\t"
    "s_add_u32 m0, %[mw], 65408\n\t"
    "s_nop 0\n\t"
    "global_load_lds_dwordx4 %[vw], s[42:43] offset:128\n\t"
    "s_add_u32 m0, %[mw], 69632\n\t"
    "s_nop 0\n\t"
    "global_load_lds_dwordx4 %[vs], off\n\t"
    "v_lshl_add_u64 %[vs], %[st], 0, %[vs]\n\t"
    "s_add_u32 m0, %[mw], 73728\n\t"
    "s_nop 0\n\t"
    "global_load_lds_dwordx4 %[vw3], s[44:45] offset:0\n\t"
    "s_add_u32 m0, %[mw], 77696\n\t"
    "s_nop 0\n\t"
    "global_load_lds_dwordx4 %[vw3], s[44:45] offset:128\n\t"
    "s_add_u32 s42, s42, %[st32]\n\t"
    "s_addc_u32 s43, s43, 0\n\t"
    "s_add_u32 s44, s44, %[st32]\n\t"
    "s_addc_u32 s45, s45, 0\n\t"
    "s_waitcnt vmcnt(21)\n\t"
    "s_barrier\n\t"
    "s_add_u32 m0, %[mx], 32768\n\t"
    "s_nop 0\n\t"
    "global_load_lds_dwordx4 %[vx0], s[40:41] sc1\n\t"
    "s_add_u32 m0, %[mx], 36864\n\t"
    "s_nop 0\n\t"
    "global_load_lds_dwordx4 %[vx1], s[40:41] sc1\n\t"
    "s_add_u32 s40, s40, 0x400\n\t"
    "s_addc_u32 s41, s41, 0\n\t"
    "s_add_u32 m0, %[mw], 81920\n\t"
    "s_nop 0\n\t"
    "global_load_lds_dwordx4 %[vw], s[42:43] offset:0\n\t"
    "s_add_u32 m0, %[mw], 85888\n\t"
    "s_nop 0\n\t"
    "global_load_lds_dwordx4 %[vw], s[42:43] offset:128\n\t"
    "s_add_u32 m0, %[mw], 90112\n\t"
    "s_nop 0\n\t"
    "global_load_lds_dwordx4 %[vs], off\n\t"
    "v_lshl_add_u64 %[vs], %[st], 0, %[vs]\n\t"
    "s_add_u32 m0, %[mw], 94208\n\t"
    "s_nop 0\n\t"
    "global_load_lds_dwordx4 %[vw3], s[44:45] offset:0\n\t"
    "s_add_u32 m0, %[mw], 98176\n\t"
    "s_nop 0\n\t"
    "global_load_lds_dwordx4 %[vw3], s[44:45] offset:128\n\t"
    "s_add_u32 s42, s42, %[st32]\n\t"
    "s_addc_u32 s43, s43, 0\n\t"
    "s_add_u32 s44, s44, %[st32]\n\t"
    "s_addc_u32 s45, s45, 0\n\t"
    "s_mov_b32 %[cnt], 2\n\t"
    "Lloop_%=:\n\t"
    "s_waitcnt vmcnt(21)\n\t"
    "s_barrier\n\t"
    "s_add_u32 m0, %[mx], 0\n\t"
    "s_nop 0\n\t"
    "global_load_lds_dwordx4 %[vx0], s[40:41] sc1\n\t"
    "s_add_u32 m0, %[mx], 4096\n\t"
    "s_nop 0\n\t"
    "global_load_lds_dwordx4 %[vx1], s[40:41] sc1\n\t"
    "s_add_u32 s40, s40, 0x400\n\t"
    "s_addc_u32 s41, s41, 0\n\t"
    "s_add_u32 m0, %[mw], 0\n\t"
    "s_nop 0\n\t"
    "global_load_lds_dwordx4 %[vw], s[42:43] offset:0\n\t"
    "s_add_u32 m0, %[mw], 3968\n\t"
    "s_nop 0\n\t"
    "global_load_lds_dwordx4 %[vw], s[42:43] offset:128\n\t"
    "s_add_u32 m0, %[mw], 8192\n\t"
    "s_nop 0\n\t"
    "global_load_lds_dwordx4 %[vs], off\n\t"
    "v_lshl_add_u64 %[vs], %[st], 0, %[vs]\n\t"
    "s_add_u32 m0, %[mw], 12288\n\t"
    "s_nop 0\n\t"
    "global_load_lds_dwordx4 %[vw3], s[44:45] offset:0\n\t"
    "s_add_u32 m0, %[mw], 16256\n\t"
    "s_nop 0\n\t"
    "global_load_lds_dwordx4 %[vw3], s[44:45] offset:128\n\t"
    "s_add_u32 s42, s42, %[st32]\n\t"
    "s_addc_u32 s43, s43, 0\n\t"
    "s_add_u32 s44, s44, %[st32]\n\t"
    "s_addc_u32 s45, s45, 0\n\t"
    "s_waitcnt vmcnt(21)\n\t"
    "s_barrier\n\t"
    "s_add_u32 m0, %[mx], 8192\n\t"
    "s_nop 0\n\t"
    "global_load_lds_dwordx4 %[vx0], s[40:41] sc1\n\t"
    "s_add_u32 m0, %[mx], 12288\n\t"
    "s_nop 0\n\t"
    "global_load_lds_dwordx4 %[vx1], s[40:41] sc1\n\t"
    "s_add_u32 s40, s40, 0x400\n\t"
    "s_addc_u32 s41, s41, 0\n\t"
    "s_add_u32 m0, %[mw], 20480\n\t"
    "s_nop 0\n\t"
    "global_load_lds_dwordx4 %[vw], s[42:43] offset:0\n\t"
    "s_add_u32 m0, %[mw], 24448\n\t"
    "s_nop 0\n\t"
    "global_load_lds_dwordx4 %[vw], s[42:43] offset:128\n\t"
    "s_add_u32 m0, %[mw], 28672\n\t"
    "s_nop 0\n\t"
    "global_load_lds_dwordx4 %[vs], off\n\t"
    "v_lshl_add_u64 %[vs], %[st], 0, %[vs]\n\t"
    "s_add_u32 m0, %[mw], 32768\n\t"
    "s_nop 0\n\t"
    "global_load_lds_dwordx4 %[vw3], s[44:45] offset:0\n\t"
    "s_add_u32 m0, %[mw], 36736\n\t"
    "s_nop 0\n\t"
    "global_load_lds_dwordx4 %[vw3], s[44:45] offset:128\n\t"
    "s_add_u32 s42, s42, %[st32]\n\t"
    "s_addc_u32 s43, s43, 0\n\t"
    "s_add_u32 s44, s44, %[st32]\n\t"
    "s_addc_u32 s45, s45, 0\n\t"
    "s_waitcnt vmcnt(21)\n\t"
    "s_barrier\n\t"
    "s_add_u32 m0, %[mx], 16384\n\t"
    "s_nop 0\n\t"
    "global_load_lds_dwordx4 %[vx0], s[40:41] sc1\n\t"
    "s_add_u32 m0, %[mx], 20480\n\t"
    "s_nop 0\n\t"
    "global_load_lds_dwordx4 %[vx1], s[40:41] sc1\n\t"
    "s_add_u32 s40, s40, 0x400\n\t"
    "s_addc_u32 s41, s41, 0\n\t"
    "s_add_u32 m0, %[mw], 40960\n\t"
    "s_nop 0\n\t"
    "global_load_lds_dwordx4 %[vw], s[42:43] offset:0\n\t"
    "s_add_u32 m0, %[mw], 44928\n\t"
    "s_nop 0\n\t"
    "global_load_lds_dwordx4 %[vw], s[42:43] offset:128\n\t"
    "s_add_u32 m0, %[mw], 49152\n\t"
    "s_nop 0\n\t"
    "global_load_lds_dwordx4 %[vs], off\n\t"
    "v_lshl_add_u64 %[vs], %[st], 0, %[vs]\n\t"
    "s_add_u32 m0, %[mw], 53248\n\t"
    "s_nop 0\n\t"
    "global_load_lds_dwordx4 %[vw3], s[44:45] offset:0\n\t"
    "s_add_u32 m0, %[mw], 57216\n\t"
    "s_nop 0\n\t"
    "global_load_lds_dwordx4 %[vw3], s[44:45] offset:128\n\t"
    "s_add_u32 s42, s42, %[st32]\n\t"
    "s_addc_u32 s43, s43, 0\n\t"
    "s_add_u32 s44, s44, %[st32]\n\t"
    "s_addc_u32 s45, s45, 0\n\t"
    "s_waitcnt vmcnt(21)\n\t"
    "s_barrier\n\t"
    "s_add_u32 m0, %[mx], 24576\n\t"
    "s_nop 0\n\t"
    "global_load_lds_dwordx4 %[vx0], s[40:41] sc1\n\t"
    "s_add_u32 m0, %[mx], 28672\n\t"
    "s_nop 0\n\t"
    "global_load_lds_dwordx4 %[vx1], s[40:41] sc1\n\t"
    "s_add_u32 s40, s40, 0x400\n\t"
    "s_addc_u32 s41, s41, 0\n\t"
    "s_add_u32 m0, %[mw], 61440\n\t"
    "s_nop 0\n\t"
    "global_load_lds_dwordx4 %[vw], s[42:43] offset:0\n\t"
    "s_add_u32 m0, %[mw], 65408\n\t"
    "s_nop 0\n\t"
    "global_load_lds_dwordx4 %[vw], s[42:43] offset:128\n\t"
    "s_add_u32 m0, %[mw], 69632\n\t"
    "s_nop 0\n\t"
    "global_load_lds_dwordx4 %[vs], off\n\t"
    "v_lshl_add_u64 %[vs], %[st], 0, %[vs]\n\t"
    "s_add_u32 m0, %[mw], 73728\n\t"
    "s_nop 0\n\t"
    "global_load_lds_dwordx4 %[vw3], s[44:45] offset:0\n\t"
    "s_add_u32 m0, %[mw], 77696\n\t"
    "s_nop 0\n\t"
    "global_load_lds_dwordx4 %[vw3], s[44:45] offset:128\n\t"
    "s_add_u32 s42, s42, %[st32]\n\t"
    "s_addc_u32 s43, s43, 0\n\t"
    "s_add_u32 s44, s44, %[st32]\n\t"
    "s_addc_u32 s45, s45, 0\n\t"
    "s_waitcnt vmcnt(21)\n\t"
    "s_barrier\n\t"
    "s_add_u32 m0, %[mx], 32768\n\t"
    "s_nop 0\n\t"
    "global_load_lds_dwordx4 %[vx0], s[40:41] sc1\n\t"
    "s_add_u32 m0, %[mx], 36864\n\t"
    "s_nop 0\n\t"
    "global_load_lds_dwordx4 %[vx1], s[40:41] sc1\n\t"
    "s_add_u32 s40, s40, 0x400\n\t"
    "s_addc_u32 s41, s41, 0\n\t"
    "s_add_u32 m0, %[mw], 81920\n\t"
    "s_nop 0\n\t"
    "global_load_lds_dwordx4 %[vw], s[42:43] offset:0\n\t"
    "s_add_u32 m0, %[mw], 85888\n\t"
    "s_nop 0\n\t"
    "global_load_lds_dwordx4 %[vw], s[42:43] offset:128\n\t"
    "s_add_u32 m0, %[mw], 90112\n\t"
    "s_nop 0\n\t"
    "global_load_lds_dwordx4 %[vs], off\n\t"
    "v_lshl_add_u64 %[vs], %[st], 0, %[vs]\n\t"
    "s_add_u32 m0, %[mw], 94208\n\t"
    "s_nop 0\n\t"
    "global_load_lds_dwordx4 %[vw3], s[44:45] offset:0\n\t"
    "s_add_u32 m0, %[mw], 98176\n\t"
    "s_nop 0\n\t"
    "global_load_lds_dwordx4 %[vw3], s[44:45] offset:128\n\t"
    "s_add_u32 s42, s42, %[st32]\n\t"
    "s_addc_u32 s43, s43, 0\n\t"
    "s_add_u32 s44, s44, %[st32]\n\t"
    "s_addc_u32 s45, s45, 0\n\t"
    "s_sub_u32 %[cnt], %[cnt], 1\n\t"
    "s_cmp_lg_u32 %[cnt], 0\n\t"
    "s_cbranch_scc1 Lloop_%=\n\t"
    "global_load_dwordx4 v[88:91], v108, %[rb]\n\t"
    "global_load_dwordx4 v[92:95], v109, %[rb]\n\t"
    "global_load_dwordx4 v[96:99], v110, %[rb]\n\t"
    "global_load_dwordx4 v[100:103], v111, %[rb]\n\t"
    "global_load_dwordx4 v[104:107], v112, %[rb]\n\t"
    "s_waitcnt vmcnt(26)\n\t"
    "s_barrier\n\t"
    "s_waitcnt vmcnt(5)\n\t"
    "s_barrier\n\t"
    "s_waitcnt vmcnt(0)\n\t"
    : "+&{s[40:41]}"(xb), "+&{s[42:43]}"(wb), "+&{s[44:45]}"(w3b), [vs] "+&v"(vs), [cnt] "=&s"(cnt), "=&{v[88:91]}"(rt[0]), "=&{v[92:95]}"(rt[1]), "=&{v[96:99]}"(rt[2]), "=&{v[100:103]}"(rt[3]), "=&{v[104:107]}"(rt[4])
    : [vx0] "v"(vx0), [vx1] "v"(vx0 + 4 * 20480), [vw] "v"(vw), [vw3] "v"(vw3), [mx] "s"(lw * 1024), [mw] "s"(40960 + lw * 1024), [st] "s"(st), [st32] "s"(stride), [ca] "v"(LDS_CNT + (vx0 & 1023) / 4), [cz] "v"(0), [rb] "s"(rb), "{v108}"(ro[0]), "{v109}"(ro[1]), "{v110}"(ro[2]), "{v111}"(ro[3]), "{v112}"(ro[4])
    : "scc", "memory");
}

__device__ __forceinline__ u64 att_main(unsigned qo, unsigned qto, unsigned akm, unsigned akt, unsigned ava, unsigned goff, u64 gbase, unsigned w, unsigned vinit,
                                        rsrc_t rs, unsigned vo, unsigned vt, unsigned so, unsigned st, unsigned fo, u64 tag) {
  unsigned cnt, t0, t1, t2, sp; u64 ok; const u64 vm = 0x000FFFFF000FFFFFull; const unsigned kh = w >> 2, qg = w & 3;
  asm volatile(
    "v_mov_b32 v64, 0\n\t"
    "v_mov_b32 v65, 0\n\t"
    "v_mov_b32 v66, 0\n\t"
    "v_mov_b32 v67, 0\n\t"
    "v_mov_b32 v68, 0\n\t"
    "v_mov_b32 v69, 0\n\t"
    "v_mov_b32 v70, 0\n\t"
    "v_mov_b32 v71, 0\n\t"
    "v_mov_b32 v72, 0\n\t"
    "v_mov_b32 v73, 0\n\t"
    "v_mov_b32 v74, 0\n\t"
    "v_mov_b32 v75, 0\n\t"
    "v_mov_b32 v76, 0\n\t"
    "v_mov_b32 v77, 0\n\t"
    "v_mov_b32 v78, 0\n\t"
    "v_mov_b32 v79, 0\n\t"
    "v_mov_b32 v80, 0\n\t"
    "v_mov_b32 v81, 0\n\t"
    "v_mov_b32 v82, 0\n\t"
    "v_mov_b32 v83, 0\n\t"
    "v_mov_b32 v84, 0\n\t"
    "v_mov_b32 v85, 0\n\t"
    "v_mov_b32 v86, 0\n\t"
    "v_mov_b32 v87, 0\n\t"
    "v_mov_b32 v88, 0\n\t"
    "v_mov_b32 v89, 0\n\t"
    "v_mov_b32 v90, 0\n\t"
    "v_mov_b32 v91, 0\n\t"
    "v_mov_b32 v92, 0\n\t"
    "v_mov_b32 v93, 0\n\t"
    "v_mov_b32 v94, 0\n\t"
    "v_mov_b32 v95, 0\n\t"
    "v_mov_b32 v56, 0\n\t"
    "v_mov_b32 v57, 0\n\t"
    "v_mov_b32 v58, 0\n\t"
    "v_mov_b32 v59, 0\n\t"
    "v_mov_b32 v60, 0\n\t"
    "v_mov_b32 v61, 0\n\t"
    "v_mov_b32 v62, 0\n\t"
    "v_mov_b32 v63, 0\n\t"
    "v_mov_b32 v110, v2\n\t"
    "v_mov_b32 v111, v2\n\t"
    "v_mov_b32 v112, v2\n\t"
    "v_mov_b32 v113, v2\n\t"
    "v_mov_b32 v114, v2\n\t"
    "v_mov_b32 v115, v2\n\t"
    "v_mov_b32 v116, v2\n\t"
    "v_mov_b32 v117, v2\n\t"
    "v_mov_b32 v122, v2\n\t"
    "v_mov_b32 v123, v2\n\t"
    "v_mov_b32 v124, v2\n\t"
    "v_mov_b32 v125, v2\n\t"
    "v_mov_b32 v126, v2\n\t"
    "v_mov_b32 v127, v2\n\t"
    "v_mov_b32 v128, v2\n\t"
    "v_mov_b32 v129, v2\n\t"
    "v_mov_b32 v134, 0xdddd\n\t"
    "v_mov_b32 v140, 1\n\t"
    "v_mov_b32 v141, 0\n\t"
    "s_mov_b32 %[sp], 0x100000\n\t"
    "s_barrier\n\t"
    "ds_read_b128 v[96:99], v0\n\t"
    "ds_read_b128 v[100:103], v0 offset:1024\n\t"
    "ds_read_b64 v[104:105], v1\n\t"
    "ds_read_b128 v[118:121], v135 offset:0\n\t"
    "ds_read_b128 v[130:133], v136 offset:0\n\t"
    "ds_read_b128 v[106:109], v135 offset:1024\n\t"
    "s_mov_b64 exec, %[vm]\n\t"
    "ds_read_b128 v[110:113], v137 offset:0\n\t"
    "s_mov_b64 exec, -1\n\t"
    "s_mov_b64 exec, %[vm]\n\t"
    "ds_read_b128 v[114:117], v137 offset:640\n\t"
    "s_mov_b64 exec, -1\n\t"
    "s_waitcnt lgkmcnt(0)\n\t"
    "v_mfma_f32_32x32x16_bf16 v[0:15], v[118:121], v[96:99], 0\n\t"
    "v_mfma_f32_32x32x16_bf16 v[16:31], v[118:121], v[100:103], 0\n\t"
    "s_nop 9\n\t"
    "v_mfma_f32_32x32x4_2b_bf16 v[0:31], v[130:131], v[104:105], v[0:31]\n\t"
    "s_nop 15\n\t"
    "s_nop 3\n\t"
    "s_mov_b32 %[cnt], 0\n\t"
    "Lloop_%=:\n\t"
    "v_exp_f32 v0, v0\n\t"
    "v_smfmac_f32_32x32x32_bf16 v[80:95], v[126:129], v[56:63], v134\n\t"
    "s_setprio 3\n\t"
    "v_readfirstlane_b32 %[t0], v141\n\t"
    "s_lshl_b32 %[t1], %[cnt], 3\n\t"
    "s_min_u32 %[t2], %[t1], 4\n\t"
    "s_add_u32 %[t1], %[t1], %[t2]\n\t"
    "s_cmp_ge_u32 %[t0], %[t1]\n\t"
    "s_cbranch_scc1 Lok0_%=\n\t"
    "Lsp0_%=:\n\t"
    "s_sleep 1\n\t"
    "ds_read_b32 v141, v139\n\t"
    "s_waitcnt lgkmcnt(0)\n\t"
    "v_readfirstlane_b32 %[t0], v141\n\t"
    "s_sub_u32 %[sp], %[sp], 1\n\t"
    "s_cbranch_scc1 Lok0_%=\n\t"
    "s_cmp_lt_u32 %[t0], %[t1]\n\t"
    "s_cbranch_scc1 Lsp0_%=\n\t"
    "Lok0_%=:\n\t"
    "ds_read_b128 v[118:121], v135 offset:5120\n\t"
    "v_exp_f32 v1, v1\n\t"
    "v_exp_f32 v2, v2\n\t"
    "v_exp_f32 v3, v3\n\t"
    "v_mfma_f32_32x32x16_bf16 v[32:47], v[106:109], v[96:99], 0\n\t"
    "s_mov_b64 exec, %[vm]\n\t"
    "ds_read_b128 v[122:125], v137 offset:1280\n\t"
    "s_mov_b64 exec, -1\n\t"
    "v_exp_f32 v4, v4\n\t"
    "v_exp_f32 v5, v5\n\t"
    "v_exp_f32 v6, v6\n\t"
    "v_exp_f32 v7, v7\n\t"
    "v_mfma_f32_32x32x16_bf16 v[48:63], v[106:109], v[100:103], 0\n\t"
    "s_mov_b64 exec, %[vm]\n\t"
    "ds_read_b128 v[126:129], v137 offset:1920\n\t"
    "s_mov_b64 exec, -1\n\t"
    "s_add_u32 m0, %[dm], 40960\n\t"
    "s_sub_u32 %[t2], %[cnt], 1\n\t"
    "v_exp_f32 v8, v8\n\t"
    "v_exp_f32 v9, v9\n\t"
    "v_exp_f32 v10, v10\n\t"
    "v_exp_f32 v11, v11\n\t"
    "v_smfmac_f32_32x32x32_bf16 v[64:79], v[110:113], v[0:7], v134\n\t"
    "s_cmp_ge_u32 %[t2], 5\n\t"
    "s_cbranch_scc1 Ls00_%=\n\t"
    "global_load_lds_dwordx4 v138, s[40:41] sc1\n\t"
    "s_add_u32 s40, s40, 8192\n\t"
    "s_addc_u32 s41, s41, 0\n\t"
    "s_add_u32 m0, %[dm], 49152\n\t"
    "Ls00_%=:\n\t"
    "v_exp_f32 v12, v12\n\t"
    "v_exp_f32 v13, v13\n\t"
    "v_exp_f32 v14, v14\n\t"
    "v_mfma_f32_32x32x4_2b_bf16 v[32:63], v[132:133], v[104:105], v[32:63]\n\t"
    "ds_read_b128 v[130:133], v136 offset:5120\n\t"
    "v_exp_f32 v15, v15\n\t"
    "v_exp_f32 v16, v16\n\t"
    "v_exp_f32 v17, v17\n\t"
    "v_exp_f32 v18, v18\n\t"
    "v_exp_f32 v19, v19\n\t"
    "v_exp_f32 v20, v20\n\t"
    "v_exp_f32 v21, v21\n\t"
    "v_exp_f32 v22, v22\n\t"
    "v_exp_f32 v23, v23\n\t"
    "v_smfmac_f32_32x32x32_bf16 v[64:79], v[114:117], v[8:15], v134\n\t"
    "s_cmp_ge_u32 %[t2], 5\n\t"
    "s_cbranch_scc1 Ls10_%=\n\t"
    "global_load_lds_dwordx4 v138, s[40:41] sc1\n\t"
    "s_add_u32 s40, s40, 8192\n\t"
    "s_addc_u32 s41, s41, 0\n\t"
    "s_add_u32 m0, %[dm], 57344\n\t"
    "Ls10_%=:\n\t"
    "v_exp_f32 v24, v24\n\t"
    "v_exp_f32 v25, v25\n\t"
    "v_exp_f32 v26, v26\n\t"
    "v_exp_f32 v27, v27\n\t"
    "v_smfmac_f32_32x32x32_bf16 v[80:95], v[110:113], v[16:23], v134\n\t"
    "s_cmp_ge_u32 %[t2], 5\n\t"
    "s_cbranch_scc1 Ls20_%=\n\t"
    "s_cmp_lg_u32 %[kh], 0\n\t"
    "s_cbranch_scc1 Ld0_%=\n\t"
    "global_load_lds_dwordx4 v138, s[40:41] sc1\n\t"
    "Ld0_%=:\n\t"
    "s_add_u32 s40, s40, 4096\n\t"
    "s_addc_u32 s41, s41, 0\n\t"
    "Ls20_%=:\n\t"
    "s_cmp_lg_u32 %[cnt], 0\n\t"
    "s_cbranch_scc1 Lh_%=\n\t"
    "s_waitcnt vmcnt(0)\n\t"
    "s_mov_b64 exec, 1\n\t"
    "ds_add_u32 v139, v140 offset:44\n\t"
    "s_mov_b64 exec, -1\n\t"
    "Lh_%=:\n\t"
    "v_exp_f32 v28, v28\n\t"
    "v_exp_f32 v29, v29\n\t"
    "v_exp_f32 v30, v30\n\t"
    "v_exp_f32 v31, v31\n\t"
    "s_waitcnt lgkmcnt(0)\n\t"
    "v_exp_f32 v32, v32\n\t"
    "v_smfmac_f32_32x32x32_bf16 v[80:95], v[114:117], v[24:31], v134\n\t"
    "s_setprio 2\n\t"
    "ds_read_b128 v[106:109], v135 offset:6144\n\t"
    "v_exp_f32 v33, v33\n\t"
    "v_exp_f32 v34, v34\n\t"
    "v_exp_f32 v35, v35\n\t"
    "v_mfma_f32_32x32x16_bf16 v[0:15], v[118:121], v[96:99], 0\n\t"
    "s_mov_b64 exec, %[vm]\n\t"
    "ds_read_b128 v[110:113], v137 offset:5120\n\t"
    "s_mov_b64 exec, -1\n\t"
    "v_exp_f32 v36, v36\n\t"
    "v_exp_f32 v37, v37\n\t"
    "v_exp_f32 v38, v38\n\t"
    "v_exp_f32 v39, v39\n\t"
    "v_mfma_f32_32x32x16_bf16 v[16:31], v[118:121], v[100:103], 0\n\t"
    "s_mov_b64 exec, %[vm]\n\t"
    "ds_read_b128 v[114:117], v137 offset:5760\n\t"
    "s_mov_b64 exec, -1\n\t"
    "v_exp_f32 v40, v40\n\t"
    "v_exp_f32 v41, v41\n\t"
    "v_exp_f32 v42, v42\n\t"
    "v_exp_f32 v43, v43\n\t"
    "v_smfmac_f32_32x32x32_bf16 v[64:79], v[122:125], v[32:39], v134\n\t"
    "v_exp_f32 v44, v44\n\t"
    "v_exp_f32 v45, v45\n\t"
    "v_exp_f32 v46, v46\n\t"
    "v_mfma_f32_32x32x4_2b_bf16 v[0:31], v[130:131], v[104:105], v[0:31]\n\t"
    "v_exp_f32 v47, v47\n\t"
    "v_exp_f32 v48, v48\n\t"
    "v_exp_f32 v49, v49\n\t"
    "v_exp_f32 v50, v50\n\t"
    "v_exp_f32 v51, v51\n\t"
    "v_exp_f32 v52, v52\n\t"
    "v_exp_f32 v53, v53\n\t"
    "v_exp_f32 v54, v54\n\t"
    "v_exp_f32 v55, v55\n\t"
    "v_smfmac_f32_32x32x32_bf16 v[64:79], v[126:129], v[40:47], v134\n\t"
    "v_exp_f32 v56, v56\n\t"
    "v_exp_f32 v57, v57\n\t"
    "v_exp_f32 v58, v58\n\t"
    "v_exp_f32 v59, v59\n\t"
    "v_smfmac_f32_32x32x32_bf16 v[80:95], v[122:125], v[48:55], v134\n\t"
    "v_exp_f32 v60, v60\n\t"
    "v_exp_f32 v61, v61\n\t"
    "v_exp_f32 v62, v62\n\t"
    "v_exp_f32 v63, v63\n\t"
    "s_waitcnt lgkmcnt(0)\n\t"
    "v_exp_f32 v0, v0\n\t"
    "v_smfmac_f32_32x32x32_bf16 v[80:95], v[126:129], v[56:63], v134\n\t"
    "s_setprio 1\n\t"
    "s_cmp_lg_u32 %[cnt], 0\n\t"
    "s_cbranch_scc1 Lc1_%=\n\t"
    "Lc1s_%=:\n\t"
    "ds_read_b32 v141, v139 offset:48\n\t"
    "s_waitcnt lgkmcnt(0)\n\t"
    "v_readfirstlane_b32 %[t0], v141\n\t"
    "s_sub_u32 %[sp], %[sp], 1\n\t"
    "s_cbranch_scc1 Lc1_%=\n\t"
    "s_cmp_lt_u32 %[t0], 4\n\t"
    "s_cbranch_scc1 Lc1s_%=\n\t"
    "Lc1_%=:\n\t"
    "ds_read_b128 v[118:121], v135 offset:20480\n\t"
    "v_exp_f32 v1, v1\n\t"
    "v_exp_f32 v2, v2\n\t"
    "v_exp_f32 v3, v3\n\t"
    "v_mfma_f32_32x32x16_bf16 v[32:47], v[106:109], v[96:99], 0\n\t"
    "s_mov_b64 exec, %[vm]\n\t"
    "ds_read_b128 v[122:125], v137 offset:6400\n\t"
    "s_mov_b64 exec, -1\n\t"
    "v_exp_f32 v4, v4\n\t"
    "v_exp_f32 v5, v5\n\t"
    "v_exp_f32 v6, v6\n\t"
    "v_exp_f32 v7, v7\n\t"
    "v_mfma_f32_32x32x16_bf16 v[48:63], v[106:109], v[100:103], 0\n\t"
    "s_mov_b64 exec, %[vm]\n\t"
    "ds_read_b128 v[126:129], v137 offset:7040\n\t"
    "s_mov_b64 exec, -1\n\t"
    "v_exp_f32 v8, v8\n\t"
    "v_exp_f32 v9, v9\n\t"
    "v_exp_f32 v10, v10\n\t"
    "v_exp_f32 v11, v11\n\t"
    "v_smfmac_f32_32x32x32_bf16 v[64:79], v[110:113], v[0:7], v134\n\t"
    "v_exp_f32 v12, v12\n\t"
    "v_exp_f32 v13, v13\n\t"
    "v_exp_f32 v14, v14\n\t"
    "v_mfma_f32_32x32x4_2b_bf16 v[32:63], v[132:133], v[104:105], v[32:63]\n\t"
    "ds_read_b128 v[130:133], v136 offset:20480\n\t"
    "v_exp_f32 v15, v15\n\t"
    "v_exp_f32 v16, v16\n\t"
    "v_exp_f32 v17, v17\n\t"
    "v_exp_f32 v18, v18\n\t"
    "v_exp_f32 v19, v19\n\t"
    "v_exp_f32 v20, v20\n\t"
    "v_exp_f32 v21, v21\n\t"
    "v_exp_f32 v22, v22\n\t"
    "v_exp_f32 v23, v23\n\t"
    "v_smfmac_f32_32x32x32_bf16 v[64:79], v[114:117], v[8:15], v134\n\t"
    "v_exp_f32 v24, v24\n\t"
    "v_exp_f32 v25, v25\n\t"
    "v_exp_f32 v26, v26\n\t"
    "v_exp_f32 v27, v27\n\t"
    "v_smfmac_f32_32x32x32_bf16 v[80:95], v[110:113], v[16:23], v134\n\t"
    "v_exp_f32 v28, v28\n\t"
    "v_exp_f32 v29, v29\n\t"
    "v_exp_f32 v30, v30\n\t"
    "v_exp_f32 v31, v31\n\t"
    "s_waitcnt lgkmcnt(0)\n\t"
    "v_exp_f32 v32, v32\n\t"
    "v_smfmac_f32_32x32x32_bf16 v[80:95], v[114:117], v[24:31], v134\n\t"
    "s_setprio 0\n\t"
    "s_waitcnt vmcnt(0)\n\t"
    "s_mov_b64 exec, 1\n\t"
    "ds_add_u32 v139, v140\n\t"
    "s_mov_b64 exec, -1\n\t"
    "ds_read_b128 v[106:109], v135 offset:21504\n\t"
    "v_exp_f32 v33, v33\n\t"
    "v_exp_f32 v34, v34\n\t"
    "v_exp_f32 v35, v35\n\t"
    "v_mfma_f32_32x32x16_bf16 v[0:15], v[118:121], v[96:99], 0\n\t"
    "s_mov_b64 exec, %[vm]\n\t"
    "ds_read_b128 v[110:113], v137 offset:20480\n\t"
    "s_mov_b64 exec, -1\n\t"
    "v_exp_f32 v36, v36\n\t"
    "v_exp_f32 v37, v37\n\t"
    "v_exp_f32 v38, v38\n\t"
    "v_exp_f32 v39, v39\n\t"
    "v_mfma_f32_32x32x16_bf16 v[16:31], v[118:121], v[100:103], 0\n\t"
    "s_mov_b64 exec, %[vm]\n\t"
    "ds_read_b128 v[114:117], v137 offset:21120\n\t"
    "s_mov_b64 exec, -1\n\t"
    "v_exp_f32 v40, v40\n\t"
    "v_exp_f32 v41, v41\n\t"
    "v_exp_f32 v42, v42\n\t"
    "v_exp_f32 v43, v43\n\t"
    "v_smfmac_f32_32x32x32_bf16 v[64:79], v[122:125], v[32:39], v134\n\t"
    "v_exp_f32 v44, v44\n\t"
    "v_exp_f32 v45, v45\n\t"
    "v_exp_f32 v46, v46\n\t"
    "v_mfma_f32_32x32x4_2b_bf16 v[0:31], v[130:131], v[104:105], v[0:31]\n\t"
    "v_exp_f32 v47, v47\n\t"
    "v_exp_f32 v48, v48\n\t"
    "v_exp_f32 v49, v49\n\t"
    "v_exp_f32 v50, v50\n\t"
    "v_exp_f32 v51, v51\n\t"
    "v_exp_f32 v52, v52\n\t"
    "v_exp_f32 v53, v53\n\t"
    "v_exp_f32 v54, v54\n\t"
    "v_exp_f32 v55, v55\n\t"
    "v_smfmac_f32_32x32x32_bf16 v[64:79], v[126:129], v[40:47], v134\n\t"
    "v_exp_f32 v56, v56\n\t"
    "v_exp_f32 v57, v57\n\t"
    "v_exp_f32 v58, v58\n\t"
    "v_exp_f32 v59, v59\n\t"
    "v_smfmac_f32_32x32x32_bf16 v[80:95], v[122:125], v[48:55], v134\n\t"
    "ds_read_b32 v141, v139\n\t"
    "v_exp_f32 v60, v60\n\t"
    "v_exp_f32 v61, v61\n\t"
    "v_exp_f32 v62, v62\n\t"
    "v_exp_f32 v63, v63\n\t"
    "s_waitcnt lgkmcnt(0)\n\t"
    "s_add_u32 %[cnt], %[cnt], 1\n\t"
    "v_exp_f32 v0, v0\n\t"
    "v_smfmac_f32_32x32x32_bf16 v[80:95], v[126:129], v[56:63], v134\n\t"
    "s_setprio 3\n\t"
    "v_readfirstlane_b32 %[t0], v141\n\t"
    "s_lshl_b32 %[t1], %[cnt], 3\n\t"
    "s_min_u32 %[t2], %[t1], 4\n\t"
    "s_add_u32 %[t1], %[t1], %[t2]\n\t"
    "s_cmp_ge_u32 %[t0], %[t1]\n\t"
    "s_cbranch_scc1 Lok1_%=\n\t"
    "Lsp1_%=:\n\t"
    "s_sleep 1\n\t"
    "ds_read_b32 v141, v139\n\t"
    "s_waitcnt lgkmcnt(0)\n\t"
    "v_readfirstlane_b32 %[t0], v141\n\t"
    "s_sub_u32 %[sp], %[sp], 1\n\t"
    "s_cbranch_scc1 Lok1_%=\n\t"
    "s_cmp_lt_u32 %[t0], %[t1]\n\t"
    "s_cbranch_scc1 Lsp1_%=\n\t"
    "Lok1_%=:\n\t"
    "ds_read_b128 v[118:121], v135 offset:25600\n\t"
    "v_exp_f32 v1, v1\n\t"
    "v_exp_f32 v2, v2\n\t"
    "v_exp_f32 v3, v3\n\t"
    "v_mfma_f32_32x32x16_bf16 v[32:47], v[106:109], v[96:99], 0\n\t"
    "s_mov_b64 exec, %[vm]\n\t"
    "ds_read_b128 v[122:125], v137 offset:21760\n\t"
    "s_mov_b64 exec, -1\n\t"
    "v_exp_f32 v4, v4\n\t"
    "v_exp_f32 v5, v5\n\t"
    "v_exp_f32 v6, v6\n\t"
    "v_exp_f32 v7, v7\n\t"
    "v_mfma_f32_32x32x16_bf16 v[48:63], v[106:109], v[100:103], 0\n\t"
    "s_mov_b64 exec, %[vm]\n\t"
    "ds_read_b128 v[126:129], v137 offset:22400\n\t"
    "s_mov_b64 exec, -1\n\t"
    "s_add_u32 m0, %[dm], 0\n\t"
    "s_sub_u32 %[t2], %[cnt], 1\n\t"
    "v_exp_f32 v8, v8\n\t"
    "v_exp_f32 v9, v9\n\t"
    "v_exp_f32 v10, v10\n\t"
    "v_exp_f32 v11, v11\n\t"
    "v_smfmac_f32_32x32x32_bf16 v[64:79], v[110:113], v[0:7], v134\n\t"
    "s_cmp_ge_u32 %[t2], 5\n\t"
    "s_cbranch_scc1 Ls01_%=\n\t"
    "global_load_lds_dwordx4 v138, s[40:41] sc1\n\t"
    "s_add_u32 s40, s40, 8192\n\t"
    "s_addc_u32 s41, s41, 0\n\t"
    "s_add_u32 m0, %[dm], 8192\n\t"
    "Ls01_%=:\n\t"
    "v_exp_f32 v12, v12\n\t"
    "v_exp_f32 v13, v13\n\t"
    "v_exp_f32 v14, v14\n\t"
    "v_mfma_f32_32x32x4_2b_bf16 v[32:63], v[132:133], v[104:105], v[32:63]\n\t"
    "ds_read_b128 v[130:133], v136 offset:25600\n\t"
    "v_exp_f32 v15, v15\n\t"
    "v_exp_f32 v16, v16\n\t"
    "v_exp_f32 v17, v17\n\t"
    "v_exp_f32 v18, v18\n\t"
    "v_exp_f32 v19, v19\n\t"
    "v_exp_f32 v20, v20\n\t"
    "v_exp_f32 v21, v21\n\t"
    "v_exp_f32 v22, v22\n\t"
    "v_exp_f32 v23, v23\n\t"
    "v_smfmac_f32_32x32x32_bf16 v[64:79], v[114:117], v[8:15], v134\n\t"
    "s_cmp_ge_u32 %[t2], 5\n\t"
    "s_cbranch_scc1 Ls11_%=\n\t"
    "global_load_lds_dwordx4 v138, s[40:41] sc1\n\t"
    "s_add_u32 s40, s40, 8192\n\t"
    "s_addc_u32 s41, s41, 0\n\t"
    "s_add_u32 m0, %[dm], 16384\n\t"
    "Ls11_%=:\n\t"
    "v_exp_f32 v24, v24\n\t"
    "v_exp_f32 v25, v25\n\t"
    "v_exp_f32 v26, v26\n\t"
    "v_exp_f32 v27, v27\n\t"
    "v_smfmac_f32_32x32x32_bf16 v[80:95], v[110:113], v[16:23], v134\n\t"
    "s_cmp_ge_u32 %[t2], 5\n\t"
    "s_cbranch_scc1 Ls21_%=\n\t"
    "s_cmp_lg_u32 %[kh], 0\n\t"
    "s_cbranch_scc1 Ld1_%=\n\t"
    "global_load_lds_dwordx4 v138, s[40:41] sc1\n\t"
    "Ld1_%=:\n\t"
    "s_add_u32 s40, s40, 4096\n\t"
    "s_addc_u32 s41, s41, 0\n\t"
    "Ls21_%=:\n\t"
    "v_exp_f32 v28, v28\n\t"
    "v_exp_f32 v29, v29\n\t"
    "v_exp_f32 v30, v30\n\t"
    "v_exp_f32 v31, v31\n\t"
    "s_waitcnt lgkmcnt(0)\n\t"
    "v_exp_f32 v32, v32\n\t"
    "v_smfmac_f32_32x32x32_bf16 v[80:95], v[114:117], v[24:31], v134\n\t"
    "s_setprio 2\n\t"
    "ds_read_b128 v[106:109], v135 offset:26624\n\t"
    "v_exp_f32 v33, v33\n\t"
    "v_exp_f32 v34, v34\n\t"
    "v_exp_f32 v35, v35\n\t"
    "v_mfma_f32_32x32x16_bf16 v[0:15], v[118:121], v[96:99], 0\n\t"
    "s_mov_b64 exec, %[vm]\n\t"
    "ds_read_b128 v[110:113], v137 offset:25600\n\t"
    "s_mov_b64 exec, -1\n\t"
    "v_exp_f32 v36, v36\n\t"
    "v_exp_f32 v37, v37\n\t"
    "v_exp_f32 v38, v38\n\t"
    "v_exp_f32 v39, v39\n\t"
    "v_mfma_f32_32x32x16_bf16 v[16:31], v[118:121], v[100:103], 0\n\t"
    "s_mov_b64 exec, %[vm]\n\t"
    "ds_read_b128 v[114:117], v137 offset:26240\n\t"
    "s_mov_b64 exec, -1\n\t"
    "v_exp_f32 v40, v40\n\t"
    "v_exp_f32 v41, v41\n\t"
    "v_exp_f32 v42, v42\n\t"
    "v_exp_f32 v43, v43\n\t"
    "v_smfmac_f32_32x32x32_bf16 v[64:79], v[122:125], v[32:39], v134\n\t"
    "v_exp_f32 v44, v44\n\t"
    "v_exp_f32 v45, v45\n\t"
    "v_exp_f32 v46, v46\n\t"
    "v_mfma_f32_32x32x4_2b_bf16 v[0:31], v[130:131], v[104:105], v[0:31]\n\t"
    "v_exp_f32 v47, v47\n\t"
    "v_exp_f32 v48, v48\n\t"
    "v_exp_f32 v49, v49\n\t"
    "v_exp_f32 v50, v50\n\t"
    "v_exp_f32 v51, v51\n\t"
    "v_exp_f32 v52, v52\n\t"
    "v_exp_f32 v53, v53\n\t"
    "v_exp_f32 v54, v54\n\t"
    "v_exp_f32 v55, v55\n\t"
    "v_smfmac_f32_32x32x32_bf16 v[64:79], v[126:129], v[40:47], v134\n\t"
    "v_exp_f32 v56, v56\n\t"
    "v_exp_f32 v57, v57\n\t"
    "v_exp_f32 v58, v58\n\t"
    "v_exp_f32 v59, v59\n\t"
    "v_smfmac_f32_32x32x32_bf16 v[80:95], v[122:125], v[48:55], v134\n\t"
    "v_exp_f32 v60, v60\n\t"
    "v_exp_f32 v61, v61\n\t"
    "v_exp_f32 v62, v62\n\t"
    "v_exp_f32 v63, v63\n\t"
    "s_waitcnt lgkmcnt(0)\n\t"
    "v_exp_f32 v0, v0\n\t"
    "v_smfmac_f32_32x32x32_bf16 v[80:95], v[126:129], v[56:63], v134\n\t"
    "s_setprio 1\n\t"
    "ds_read_b128 v[118:121], v135 offset:40960\n\t"
    "v_exp_f32 v1, v1\n\t"
    "v_exp_f32 v2, v2\n\t"
    "v_exp_f32 v3, v3\n\t"
    "v_mfma_f32_32x32x16_bf16 v[32:47], v[106:109], v[96:99], 0\n\t"
    "s_mov_b64 exec, %[vm]\n\t"
    "ds_read_b128 v[122:125], v137 offset:26880\n\t"
    "s_mov_b64 exec, -1\n\t"
    "v_exp_f32 v4, v4\n\t"
    "v_exp_f32 v5, v5\n\t"
    "v_exp_f32 v6, v6\n\t"
    "v_exp_f32 v7, v7\n\t"
    "v_mfma_f32_32x32x16_bf16 v[48:63], v[106:109], v[100:103], 0\n\t"
    "s_mov_b64 exec, %[vm]\n\t"
    "ds_read_b128 v[126:129], v137 offset:27520\n\t"
    "s_mov_b64 exec, -1\n\t"
    "v_exp_f32 v8, v8\n\t"
    "v_exp_f32 v9, v9\n\t"
    "v_exp_f32 v10, v10\n\t"
    "v_exp_f32 v11, v11\n\t"
    "v_smfmac_f32_32x32x32_bf16 v[64:79], v[110:113], v[0:7], v134\n\t"
    "v_exp_f32 v12, v12\n\t"
    "v_exp_f32 v13, v13\n\t"
    "v_exp_f32 v14, v14\n\t"
    "v_mfma_f32_32x32x4_2b_bf16 v[32:63], v[132:133], v[104:105], v[32:63]\n\t"
    "ds_read_b128 v[130:133], v136 offset:40960\n\t"
    "v_exp_f32 v15, v15\n\t"
    "v_exp_f32 v16, v16\n\t"
    "v_exp_f32 v17, v17\n\t"
    "v_exp_f32 v18, v18\n\t"
    "v_exp_f32 v19, v19\n\t"
    "v_exp_f32 v20, v20\n\t"
    "v_exp_f32 v21, v21\n\t"
    "v_exp_f32 v22, v22\n\t"
    "v_exp_f32 v23, v23\n\t"
    "v_smfmac_f32_32x32x32_bf16 v[64:79], v[114:117], v[8:15], v134\n\t"
    "v_exp_f32 v24, v24\n\t"
    "v_exp_f32 v25, v25\n\t"
    "v_exp_f32 v26, v26\n\t"
    "v_exp_f32 v27, v27\n\t"
    "v_smfmac_f32_32x32x32_bf16 v[80:95], v[110:113], v[16:23], v134\n\t"
    "v_exp_f32 v28, v28\n\t"
    "v_exp_f32 v29, v29\n\t"
    "v_exp_f32 v30, v30\n\t"
    "v_exp_f32 v31, v31\n\t"
    "s_waitcnt lgkmcnt(0)\n\t"
    "v_exp_f32 v32, v32\n\t"
    "v_smfmac_f32_32x32x32_bf16 v[80:95], v[114:117], v[24:31], v134\n\t"
    "s_setprio 0\n\t"
    "s_waitcnt vmcnt(0)\n\t"
    "s_mov_b64 exec, 1\n\t"
    "ds_add_u32 v139, v140\n\t"
    "s_mov_b64 exec, -1\n\t"
    "ds_read_b128 v[106:109], v135 offset:41984\n\t"
    "v_exp_f32 v33, v33\n\t"
    "v_exp_f32 v34, v34\n\t"
    "v_exp_f32 v35, v35\n\t"
    "v_mfma_f32_32x32x16_bf16 v[0:15], v[118:121], v[96:99], 0\n\t"
    "s_mov_b64 exec, %[vm]\n\t"
    "ds_read_b128 v[110:113], v137 offset:40960\n\t"
    "s_mov_b64 exec, -1\n\t"
    "v_exp_f32 v36, v36\n\t"
    "v_exp_f32 v37, v37\n\t"
    "v_exp_f32 v38, v38\n\t"
    "v_exp_f32 v39, v39\n\t"
    "v_mfma_f32_32x32x16_bf16 v[16:31], v[118:121], v[100:103], 0\n\t"
    "s_mov_b64 exec, %[vm]\n\t"
    "ds_read_b128 v[114:117], v137 offset:41600\n\t"
    "s_mov_b64 exec, -1\n\t"
    "v_exp_f32 v40, v40\n\t"
    "v_exp_f32 v41, v41\n\t"
    "v_exp_f32 v42, v42\n\t"
    "v_exp_f32 v43, v43\n\t"
    "v_smfmac_f32_32x32x32_bf16 v[64:79], v[122:125], v[32:39], v134\n\t"
    "v_exp_f32 v44, v44\n\t"
    "v_exp_f32 v45, v45\n\t"
    "v_exp_f32 v46, v46\n\t"
    "v_mfma_f32_32x32x4_2b_bf16 v[0:31], v[130:131], v[104:105], v[0:31]\n\t"
    "v_exp_f32 v47, v47\n\t"
    "v_exp_f32 v48, v48\n\t"
    "v_exp_f32 v49, v49\n\t"
    "v_exp_f32 v50, v50\n\t"
    "v_exp_f32 v51, v51\n\t"
    "v_exp_f32 v52, v52\n\t"
    "v_exp_f32 v53, v53\n\t"
    "v_exp_f32 v54, v54\n\t"
    "v_exp_f32 v55, v55\n\t"
    "v_smfmac_f32_32x32x32_bf16 v[64:79], v[126:129], v[40:47], v134\n\t"
    "v_exp_f32 v56, v56\n\t"
    "v_exp_f32 v57, v57\n\t"
    "v_exp_f32 v58, v58\n\t"
    "v_exp_f32 v59, v59\n\t"
    "v_smfmac_f32_32x32x32_bf16 v[80:95], v[122:125], v[48:55], v134\n\t"
    "ds_read_b32 v141, v139\n\t"
    "v_exp_f32 v60, v60\n\t"
    "v_exp_f32 v61, v61\n\t"
    "v_exp_f32 v62, v62\n\t"
    "v_exp_f32 v63, v63\n\t"
    "s_waitcnt lgkmcnt(0)\n\t"
    "s_add_u32 %[cnt], %[cnt], 1\n\t"
    "s_cmp_eq_u32 %[cnt], 8\n\t"
    "s_cbranch_scc1 Ldone_%=\n\t"
    "v_exp_f32 v0, v0\n\t"
    "v_smfmac_f32_32x32x32_bf16 v[80:95], v[126:129], v[56:63], v134\n\t"
    "s_setprio 3\n\t"
    "v_readfirstlane_b32 %[t0], v141\n\t"
    "s_lshl_b32 %[t1], %[cnt], 3\n\t"
    "s_min_u32 %[t2], %[t1], 4\n\t"
    "s_add_u32 %[t1], %[t1], %[t2]\n\t"
    "s_cmp_ge_u32 %[t0], %[t1]\n\t"
    "s_cbranch_scc1 Lok2_%=\n\t"
    "Lsp2_%=:\n\t"
    "s_sleep 1\n\t"
    "ds_read_b32 v141, v139\n\t"
    "s_waitcnt lgkmcnt(0)\n\t"
    "v_readfirstlane_b32 %[t0], v141\n\t"
    "s_sub_u32 %[sp], %[sp], 1\n\t"
    "s_cbranch_scc1 Lok2_%=\n\t"
    "s_cmp_lt_u32 %[t0], %[t1]\n\t"
    "s_cbranch_scc1 Lsp2_%=\n\t"
    "Lok2_%=:\n\t"
    "ds_read_b128 v[118:121], v135 offset:46080\n\t"
    "v_exp_f32 v1, v1\n\t"
    "v_exp_f32 v2, v2\n\t"
    "v_exp_f32 v3, v3\n\t"
    "v_mfma_f32_32x32x16_bf16 v[32:47], v[106:109], v[96:99], 0\n\t"
    "s_mov_b64 exec, %[vm]\n\t"
    "ds_read_b128 v[122:125], v137 offset:42240\n\t"
    "s_mov_b64 exec, -1\n\t"
    "v_exp_f32 v4, v4\n\t"
    "v_exp_f32 v5, v5\n\t"
    "v_exp_f32 v6, v6\n\t"
    "v_exp_f32 v7, v7\n\t"
    "v_mfma_f32_32x32x16_bf16 v[48:63], v[106:109], v[100:103], 0\n\t"
    "s_mov_b64 exec, %[vm]\n\t"
    "ds_read_b128 v[126:129], v137 offset:42880\n\t"
    "s_mov_b64 exec, -1\n\t"
    "s_add_u32 m0, %[dm], 20480\n\t"
    "s_sub_u32 %[t2], %[cnt], 1\n\t"
    "v_exp_f32 v8, v8\n\t"
    "v_exp_f32 v9, v9\n\t"
    "v_exp_f32 v10, v10\n\t"
    "v_exp_f32 v11, v11\n\t"
    "v_smfmac_f32_32x32x32_bf16 v[64:79], v[110:113], v[0:7], v134\n\t"
    "s_cmp_ge_u32 %[t2], 5\n\t"
    "s_cbranch_scc1 Ls02_%=\n\t"
    "global_load_lds_dwordx4 v138, s[40:41] sc1\n\t"
    "s_add_u32 s40, s40, 8192\n\t"
    "s_addc_u32 s41, s41, 0\n\t"
    "s_add_u32 m0, %[dm], 28672\n\t"
    "Ls02_%=:\n\t"
    "v_exp_f32 v12, v12\n\t"
    "v_exp_f32 v13, v13\n\t"
    "v_exp_f32 v14, v14\n\t"
    "v_mfma_f32_32x32x4_2b_bf16 v[32:63], v[132:133], v[104:105], v[32:63]\n\t"
    "ds_read_b128 v[130:133], v136 offset:46080\n\t"
    "v_exp_f32 v15, v15\n\t"
    "v_exp_f32 v16, v16\n\t"
    "v_exp_f32 v17, v17\n\t"
    "v_exp_f32 v18, v18\n\t"
    "v_exp_f32 v19, v19\n\t"
    "v_exp_f32 v20, v20\n\t"
    "v_exp_f32 v21, v21\n\t"
    "v_exp_f32 v22, v22\n\t"
    "v_exp_f32 v23, v23\n\t"
    "v_smfmac_f32_32x32x32_bf16 v[64:79], v[114:117], v[8:15], v134\n\t"
    "s_cmp_ge_u32 %[t2], 5\n\t"
    "s_cbranch_scc1 Ls12_%=\n\t"
    "global_load_lds_dwordx4 v138, s[40:41] sc1\n\t"
    "s_add_u32 s40, s40, 8192\n\t"
    "s_addc_u32 s41, s41, 0\n\t"
    "s_add_u32 m0, %[dm], 36864\n\t"
    "Ls12_%=:\n\t"
    "v_exp_f32 v24, v24\n\t"
    "v_exp_f32 v25, v25\n\t"
    "v_exp_f32 v26, v26\n\t"
    "v_exp_f32 v27, v27\n\t"
    "v_smfmac_f32_32x32x32_bf16 v[80:95], v[110:113], v[16:23], v134\n\t"
    "s_cmp_ge_u32 %[t2], 5\n\t"
    "s_cbranch_scc1 Ls22_%=\n\t"
    "s_cmp_lg_u32 %[kh], 0\n\t"
    "s_cbranch_scc1 Ld2_%=\n\t"
    "global_load_lds_dwordx4 v138, s[40:41] sc1\n\t"
    "Ld2_%=:\n\t"
    "s_add_u32 s40, s40, 4096\n\t"
    "s_addc_u32 s41, s41, 0\n\t"
    "Ls22_%=:\n\t"
    "v_exp_f32 v28, v28\n\t"
    "v_exp_f32 v29, v29\n\t"
    "v_exp_f32 v30, v30\n\t"
    "v_exp_f32 v31, v31\n\t"
    "s_waitcnt lgkmcnt(0)\n\t"
    "v_exp_f32 v32, v32\n\t"
    "v_smfmac_f32_32x32x32_bf16 v[80:95], v[114:117], v[24:31], v134\n\t"
    "s_setprio 2\n\t"
    "ds_read_b128 v[106:109], v135 offset:47104\n\t"
    "v_exp_f32 v33, v33\n\t"
    "v_exp_f32 v34, v34\n\t"
    "v_exp_f32 v35, v35\n\t"
    "v_mfma_f32_32x32x16_bf16 v[0:15], v[118:121], v[96:99], 0\n\t"
    "s_mov_b64 exec, %[vm]\n\t"
    "ds_read_b128 v[110:113], v137 offset:46080\n\t"
    "s_mov_b64 exec, -1\n\t"
    "v_exp_f32 v36, v36\n\t"
    "v_exp_f32 v37, v37\n\t"
    "v_exp_f32 v38, v38\n\t"
    "v_exp_f32 v39, v39\n\t"
    "v_mfma_f32_32x32x16_bf16 v[16:31], v[118:121], v[100:103], 0\n\t"
    "s_mov_b64 exec, %[vm]\n\t"
    "ds_read_b128 v[114:117], v137 offset:46720\n\t"
    "s_mov_b64 exec, -1\n\t"
    "v_exp_f32 v40, v40\n\t"
    "v_exp_f32 v41, v41\n\t"
    "v_exp_f32 v42, v42\n\t"
    "v_exp_f32 v43, v43\n\t"
    "v_smfmac_f32_32x32x32_bf16 v[64:79], v[122:125], v[32:39], v134\n\t"
    "v_exp_f32 v44, v44\n\t"
    "v_exp_f32 v45, v45\n\t"
    "v_exp_f32 v46, v46\n\t"
    "v_mfma_f32_32x32x4_2b_bf16 v[0:31], v[130:131], v[104:105], v[0:31]\n\t"
    "v_exp_f32 v47, v47\n\t"
    "v_exp_f32 v48, v48\n\t"
    "v_exp_f32 v49, v49\n\t"
    "v_exp_f32 v50, v50\n\t"
    "v_exp_f32 v51, v51\n\t"
    "v_exp_f32 v52, v52\n\t"
    "v_exp_f32 v53, v53\n\t"
    "v_exp_f32 v54, v54\n\t"
    "v_exp_f32 v55, v55\n\t"
    "v_smfmac_f32_32x32x32_bf16 v[64:79], v[126:129], v[40:47], v134\n\t"
    "v_exp_f32 v56, v56\n\t"
    "v_exp_f32 v57, v57\n\t"
    "v_exp_f32 v58, v58\n\t"
    "v_exp_f32 v59, v59\n\t"
    "v_smfmac_f32_32x32x32_bf16 v[80:95], v[122:125], v[48:55], v134\n\t"
    "v_exp_f32 v60, v60\n\t"
    "v_exp_f32 v61, v61\n\t"
    "v_exp_f32 v62, v62\n\t"
    "v_exp_f32 v63, v63\n\t"
    "s_waitcnt lgkmcnt(0)\n\t"
    "v_exp_f32 v0, v0\n\t"
    "v_smfmac_f32_32x32x32_bf16 v[80:95], v[126:129], v[56:63], v134\n\t"
    "s_setprio 1\n\t"
    "ds_read_b128 v[118:121], v135 offset:0\n\t"
    "v_exp_f32 v1, v1\n\t"
    "v_exp_f32 v2, v2\n\t"
    "v_exp_f32 v3, v3\n\t"
    "v_mfma_f32_32x32x16_bf16 v[32:47], v[106:109], v[96:99], 0\n\t"
    "s_mov_b64 exec, %[vm]\n\t"
    "ds_read_b128 v[122:125], v137 offset:47360\n\t"
    "s_mov_b64 exec, -1\n\t"
    "v_exp_f32 v4, v4\n\t"
    "v_exp_f32 v5, v5\n\t"
    "v_exp_f32 v6, v6\n\t"
    "v_exp_f32 v7, v7\n\t"
    "v_mfma_f32_32x32x16_bf16 v[48:63], v[106:109], v[100:103], 0\n\t"
    "s_mov_b64 exec, %[vm]\n\t"
    "ds_read_b128 v[126:129], v137 offset:48000\n\t"
    "s_mov_b64 exec, -1\n\t"
    "v_exp_f32 v8, v8\n\t"
    "v_exp_f32 v9, v9\n\t"
    "v_exp_f32 v10, v10\n\t"
    "v_exp_f32 v11, v11\n\t"
    "v_smfmac_f32_32x32x32_bf16 v[64:79], v[110:113], v[0:7], v134\n\t"
    "v_exp_f32 v12, v12\n\t"
    "v_exp_f32 v13, v13\n\t"
    "v_exp_f32 v14, v14\n\t"
    "v_mfma_f32_32x32x4_2b_bf16 v[32:63], v[132:133], v[104:105], v[32:63]\n\t"
    "ds_read_b128 v[130:133], v136 offset:0\n\t"
    "v_exp_f32 v15, v15\n\t"
    "v_exp_f32 v16, v16\n\t"
    "v_exp_f32 v17, v17\n\t"
    "v_exp_f32 v18, v18\n\t"
    "v_exp_f32 v19, v19\n\t"
    "v_exp_f32 v20, v20\n\t"
    "v_exp_f32 v21, v21\n\t"
    "v_exp_f32 v22, v22\n\t"
    "v_exp_f32 v23, v23\n\t"
    "v_smfmac_f32_32x32x32_bf16 v[64:79], v[114:117], v[8:15], v134\n\t"
    "v_exp_f32 v24, v24\n\t"
    "v_exp_f32 v25, v25\n\t"
    "v_exp_f32 v26, v26\n\t"
    "v_exp_f32 v27, v27\n\t"
    "v_smfmac_f32_32x32x32_bf16 v[80:95], v[110:113], v[16:23], v134\n\t"
    "v_exp_f32 v28, v28\n\t"
    "v_exp_f32 v29, v29\n\t"
    "v_exp_f32 v30, v30\n\t"
    "v_exp_f32 v31, v31\n\t"
    "s_waitcnt lgkmcnt(0)\n\t"
    "v_exp_f32 v32, v32\n\t"
    "v_smfmac_f32_32x32x32_bf16 v[80:95], v[114:117], v[24:31], v134\n\t"
    "s_setprio 0\n\t"
    "s_waitcnt vmcnt(0)\n\t"
    "s_mov_b64 exec, 1\n\t"
    "ds_add_u32 v139, v140\n\t"
    "s_mov_b64 exec, -1\n\t"
    "ds_read_b128 v[106:109], v135 offset:1024\n\t"
    "v_exp_f32 v33, v33\n\t"
    "v_exp_f32 v34, v34\n\t"
    "v_exp_f32 v35, v35\n\t"
    "v_mfma_f32_32x32x16_bf16 v[0:15], v[118:121], v[96:99], 0\n\t"
    "s_mov_b64 exec, %[vm]\n\t"
    "ds_read_b128 v[110:113], v137 offset:0\n\t"
    "s_mov_b64 exec, -1\n\t"
    "v_exp_f32 v36, v36\n\t"
    "v_exp_f32 v37, v37\n\t"
    "v_exp_f32 v38, v38\n\t"
    "v_exp_f32 v39, v39\n\t"
    "v_mfma_f32_32x32x16_bf16 v[16:31], v[118:121], v[100:103], 0\n\t"
    "s_mov_b64 exec, %[vm]\n\t"
    "ds_read_b128 v[114:117], v137 offset:640\n\t"
    "s_mov_b64 exec, -1\n\t"
    "v_exp_f32 v40, v40\n\t"
    "v_exp_f32 v41, v41\n\t"
    "v_exp_f32 v42, v42\n\t"
    "v_exp_f32 v43, v43\n\t"
    "v_smfmac_f32_32x32x32_bf16 v[64:79], v[122:125], v[32:39], v134\n\t"
    "v_exp_f32 v44, v44\n\t"
    "v_exp_f32 v45, v45\n\t"
    "v_exp_f32 v46, v46\n\t"
    "v_mfma_f32_32x32x4_2b_bf16 v[0:31], v[130:131], v[104:105], v[0:31]\n\t"
    "v_exp_f32 v47, v47\n\t"
    "v_exp_f32 v48, v48\n\t"
    "v_exp_f32 v49, v49\n\t"
    "v_exp_f32 v50, v50\n\t"
    "v_exp_f32 v51, v51\n\t"
    "v_exp_f32 v52, v52\n\t"
    "v_exp_f32 v53, v53\n\t"
    "v_exp_f32 v54, v54\n\t"
    "v_exp_f32 v55, v55\n\t"
    "v_smfmac_f32_32x32x32_bf16 v[64:79], v[126:129], v[40:47], v134\n\t"
    "v_exp_f32 v56, v56\n\t"
    "v_exp_f32 v57, v57\n\t"
    "v_exp_f32 v58, v58\n\t"
    "v_exp_f32 v59, v59\n\t"
    "v_smfmac_f32_32x32x32_bf16 v[80:95], v[122:125], v[48:55], v134\n\t"
    "ds_read_b32 v141, v139\n\t"
    "v_exp_f32 v60, v60\n\t"
    "v_exp_f32 v61, v61\n\t"
    "v_exp_f32 v62, v62\n\t"
    "v_exp_f32 v63, v63\n\t"
    "s_waitcnt lgkmcnt(0)\n\t"
    "s_add_u32 %[cnt], %[cnt], 1\n\t"
    "s_branch Lloop_%=\n\t"
    "Ldone_%=:\n\t"
    "s_nop 1\n\t"
    "v_smfmac_f32_32x32x32_bf16 v[80:95], v[126:129], v[56:63], v134\n\t"
    "v_mbcnt_lo_u32_b32 v0, -1, 0\n\t"
    "v_mbcnt_hi_u32_b32 v0, -1, v0\n\t"
    "v_lshlrev_b32 v0, 4, v0\n\t"
    "v_add_u32 v1, %[xr], v0\n\t"
    "v_add_u32 v0, %[xw], v0\n\t"
    "v_mov_b32 v2, %[mw]\n\t"
    "v_mov_b32 v3, %[mr]\n\t"
    "s_setprio 3\n\t"
    "s_cmp_lg_u32 %[kh], 0\n\t"
    "s_cbranch_scc1 Lg1_%=\n\t"
    "s_nop 4\n\t"
    "ds_write_b128 v0, v[80:83] offset:0\n\t"
    "ds_write_b128 v0, v[84:87] offset:1024\n\t"
    "ds_write_b128 v0, v[88:91] offset:2048\n\t"
    "ds_write_b128 v0, v[92:95] offset:3072\n\t"
    "s_branch Lg2_%=\n\t"
    "Lg1_%=:\n\t"
    "ds_write_b128 v0, v[64:67] offset:0\n\t"
    "ds_write_b128 v0, v[68:71] offset:1024\n\t"
    "ds_write_b128 v0, v[72:75] offset:2048\n\t"
    "ds_write_b128 v0, v[76:79] offset:3072\n\t"
    "Lg2_%=:\n\t"
    "s_waitcnt lgkmcnt(0)\n\t"
    "s_mov_b64 exec, 1\n\t"
    "ds_add_u32 v2, v140\n\t"
    "s_mov_b64 exec, -1\n\t"
    "Lx_%=:\n\t"
    "ds_read_b32 v4, v3\n\t"
    "s_waitcnt lgkmcnt(0)\n\t"
    "v_readfirstlane_b32 %[t0], v4\n\t"
    "s_cmp_lg_u32 %[t0], 0\n\t"
    "s_cbranch_scc1 Lxd_%=\n\t"
    "s_sub_u32 %[sp], %[sp], 1\n\t"
    "s_cbranch_scc0 Lx_%=\n\t"
    "Lxd_%=:\n\t"
    "ds_read_b128 v[8:11], v1 offset:0\n\t"
    "ds_read_b128 v[12:15], v1 offset:1024\n\t"
    "ds_read_b128 v[16:19], v1 offset:2048\n\t"
    "ds_read_b128 v[20:23], v1 offset:3072\n\t"
    "s_mov_b32 vcc_lo, 0\n\t"
    "s_mov_b32 vcc_hi, -1\n\t"
    "s_cmp_lg_u32 %[kh], 0\n\t"
    "s_waitcnt lgkmcnt(0)\n\t"
    "s_cbranch_scc1 Lg3_%=\n\t"
    "v_pk_add_f32 v[32:33], v[64:65], v[8:9]\n\t"
    "v_pk_add_f32 v[34:35], v[66:67], v[10:11]\n\t"
    "v_pk_add_f32 v[36:37], v[68:69], v[12:13]\n\t"
    "v_pk_add_f32 v[38:39], v[70:71], v[14:15]\n\t"
    "v_pk_add_f32 v[40:41], v[72:73], v[16:17]\n\t"
    "v_pk_add_f32 v[42:43], v[74:75], v[18:19]\n\t"
    "v_pk_add_f32 v[44:45], v[76:77], v[20:21]\n\t"
    "s_branch Lg4_%=\n\t"
    "Lg3_%=:\n\t"
    "v_pk_add_f32 v[32:33], v[80:81], v[8:9]\n\t"
    "v_pk_add_f32 v[34:35], v[82:83], v[10:11]\n\t"
    "v_pk_add_f32 v[36:37], v[84:85], v[12:13]\n\t"
    "v_pk_add_f32 v[38:39], v[86:87], v[14:15]\n\t"
    "v_pk_add_f32 v[40:41], v[88:89], v[16:17]\n\t"
    "v_pk_add_f32 v[42:43], v[90:91], v[18:19]\n\t"
    "v_pk_add_f32 v[44:45], v[92:93], v[20:21]\n\t"
    "Lg4_%=:\n\t"
    "v_cndmask_b32 v4, v44, v40, vcc\n\t"
    "v_pk_add_f32 v[8:9], v[32:33], v[34:35]\n\t"
    "v_pk_add_f32 v[10:11], v[36:37], v[38:39]\n\t"
    "v_pk_add_f32 v[12:13], v[40:41], v[42:43]\n\t"
    "v_rcp_f32 v6, v4\n\t"
    "v_pk_add_f32 v[8:9], v[8:9], v[10:11]\n\t"
    "v_pk_add_f32 v[8:9], v[8:9], v[12:13]\n\t"
    "v_add_f32 v5, v8, v9\n\t"
    "v_cmp_lt_f32 vcc, 0x2b800000, v4\n\t"
    "s_mov_b64 %[ok], vcc\n\t"
    "v_cmp_gt_f32 vcc, 0x5d800000, v4\n\t"
    "s_and_b64 %[ok], %[ok], vcc\n\t"
    "v_cmp_class_f32 vcc, v5, %[fin]\n\t"
    "s_and_b64 %[ok], %[ok], vcc\n\t"
    "s_cmp_eq_u64 %[ok], -1\n\t"
    "s_cbranch_scc0 Lfin_%=\n\t"
    "v_pk_mul_f32 v[8:9], v[32:33], v[6:7] op_sel_hi:[1,0]\n\t"
    "v_pk_mul_f32 v[10:11], v[34:35], v[6:7] op_sel_hi:[1,0]\n\t"
    "v_pk_mul_f32 v[12:13], v[36:37], v[6:7] op_sel_hi:[1,0]\n\t"
    "v_pk_mul_f32 v[14:15], v[38:39], v[6:7] op_sel_hi:[1,0]\n\t"
    "v_pk_mul_f32 v[16:17], v[40:41], v[6:7] op_sel_hi:[1,0]\n\t"
    "v_pk_mul_f32 v[18:19], v[42:43], v[6:7] op_sel_hi:[1,0]\n\t"
    "v_cvt_pk_bf16_f32 v24, v8, v9\n\t"
    "v_cvt_pk_bf16_f32 v25, v10, v11\n\t"
    "v_cvt_pk_bf16_f32 v26, v12, v13\n\t"
    "v_cvt_pk_bf16_f32 v27, v14, v15\n\t"
    "v_cvt_pk_bf16_f32 v28, v16, v17\n\t"
    "v_cvt_pk_bf16_f32 v29, v18, v19\n\t"
    "buffer_store_dwordx4 v[24:27], v142, %[rs], %[so] offen sc1\n\t"
    "s_mov_b64 exec, 0xffffffff\n\t"
    "buffer_store_dwordx2 v[28:29], v143, %[rs], %[st] offen sc1\n\t"
    "s_mov_b64 exec, 1\n\t"
    "ds_add_u32 v139, v140 offset:8\n\t"
    "v_mov_b32 v8, %[tl]\n\t"
    "v_mov_b32 v9, %[th]\n\t"
    "v_mov_b32 v10, 0\n\t"
    "s_waitcnt vmcnt(0)\n\t"
    "buffer_store_dwordx2 v[8:9], v10, %[rs], %[fo] offen sc1\n\t"
    "s_mov_b64 exec, -1\n\t"
    "Lfin_%=:\n\t"
    : [ok] "=&s"(ok), "+&{s[40:41]}"(gbase), "+{v0}"(qo), "+{v1}"(qto), "+{v2}"(vinit), [cnt] "=&s"(cnt), [t0] "=&s"(t0), [t1] "=&s"(t1), [t2] "=&s"(t2), [sp] "=&s"(sp)
    : "{v135}"(akm), "{v136}"(akt), "{v137}"(ava), "{v138}"(goff), "{v139}"(LDS_CNT + 4),
      "{v142}"(vo), "{v143}"(vt), [rs] "s"(rs), [so] "s"(so), [st] "s"(st), [fo] "s"(fo), [tl] "s"((unsigned)tag), [th] "s"((unsigned)(tag >> 32)),
      [vm] "s"(vm), [kh] "s"(kh), [fin] "s"(0x1f8), [dm] "s"(LDS_KV + w * 1024), [xw] "s"(LDS_KV + kh * 40960 + qg * 4096), [xr] "s"(LDS_KV + (kh ^ 1) * 40960 + qg * 4096),
      [mw] "s"(LDS_CNT + 16 + 4 * w), [mr] "s"(LDS_CNT + 16 + 4 * (w ^ 4))
    : "v3", "v4", "v5", "v6", "v7", "v8", "v9", "v10", "v11", "v12", "v13", "v14", "v15", "v16", "v17", "v18", "v19", "v20", "v21", "v22", "v23", "v24", "v25", "v26", "v27", "v28", "v29", "v30", "v31", "v32", "v33", "v34", "v35", "v36", "v37", "v38", "v39", "v40", "v41", "v42", "v43", "v44", "v45", "v46", "v47", "v48", "v49", "v50", "v51", "v52", "v53", "v54", "v55", "v56", "v57", "v58", "v59", "v60", "v61", "v62", "v63", "v64", "v65", "v66", "v67", "v68", "v69", "v70", "v71", "v72", "v73", "v74", "v75", "v76", "v77", "v78", "v79", "v80", "v81", "v82", "v83", "v84", "v85", "v86", "v87", "v88", "v89", "v90", "v91", "v92", "v93", "v94", "v95", "v96", "v97", "v98", "v99", "v100", "v101", "v102", "v103", "v104", "v105", "v106", "v107", "v108", "v109", "v110", "v111", "v112", "v113", "v114", "v115", "v116", "v117", "v118", "v119", "v120", "v121", "v122", "v123", "v124", "v125", "v126", "v127", "v128", "v129", "v130", "v131", "v132", "v133", "v134", "v140", "v141", "vcc", "scc", "memory");
  return ok;
}

__device__ __forceinline__ void ffn_tail(char* ws, rsrc_t rs, u64 tag, unsigned kq, unsigned p1, unsigned p2, unsigned p3, unsigned p4, unsigned pf2, u32x4 (&rt)[5], unsigned rto, unsigned rfo,
                                         unsigned h0, unsigned h1, unsigned h2, unsigned h3, unsigned l16, unsigned ot, unsigned xo,
                                         u64 kv, u64 qb, unsigned pub, unsigned epo, u64 wt, u64 hb, u64 wb, u64 xb, u64 w0, u64 ob, u64 tb, u64 out) {
  unsigned cnt, sp, t0, ot2 = ot + 16384; const unsigned la = l16, lb = l16 + LDS_HI;
  asm volatile(
    "s_nop 4\n\t"
    "s_mov_b32 %[sp], 0x100000\n\t"
    "global_load_dwordx2 v[20:21], v159, %[xb] offset:0\n\t"
    "global_load_dwordx2 v[22:23], v159, %[xb] offset:32\n\t"
    "global_load_dwordx2 v[24:25], v159, %[xb] offset:64\n\t"
    "global_load_dwordx2 v[26:27], v159, %[xb] offset:96\n\t"
    "global_load_dwordx2 v[28:29], v159, %[xb] offset:128\n\t"
    "v_mov_b32 v30, 0\n\t"
    "v_mov_b32 v31, 0\n\t"
    "v_mov_b32 v32, 0\n\t"
    "v_mov_b32 v33, 0\n\t"
    "v_mov_b32 v34, 0\n\t"
    "v_mov_b32 v35, 0\n\t"
    "v_mov_b32 v36, 0\n\t"
    "v_mov_b32 v37, 0\n\t"
    "v_mov_b32 v38, 0\n\t"
    "v_mov_b32 v39, 0\n\t"
    "v_mov_b32 v40, 0\n\t"
    "v_mov_b32 v41, 0\n\t"
    "v_mov_b32 v42, 0\n\t"
    "v_mov_b32 v43, 0\n\t"
    "v_mov_b32 v44, 0\n\t"
    "v_mov_b32 v45, 0\n\t"
    "v_mov_b32 v46, 0\n\t"
    "v_mov_b32 v47, 0\n\t"
    "v_mov_b32 v48, 0\n\t"
    "v_mov_b32 v49, 0\n\t"
    "v_mov_b32 v50, 0\n\t"
    "v_mov_b32 v51, 0\n\t"
    "v_mov_b32 v52, 0\n\t"
    "v_mov_b32 v53, 0\n\t"
    "v_mov_b32 v54, 0\n\t"
    "v_mov_b32 v55, 0\n\t"
    "v_mov_b32 v56, 0\n\t"
    "v_mov_b32 v57, 0\n\t"
    "v_mov_b32 v58, 0\n\t"
    "v_mov_b32 v59, 0\n\t"
    "v_mov_b32 v60, 0\n\t"
    "v_mov_b32 v61, 0\n\t"
    "v_mov_b32 v62, 0\n\t"
    "v_mov_b32 v63, 0\n\t"
    "v_mov_b32 v64, 0\n\t"
    "v_mov_b32 v65, 0\n\t"
    "v_mov_b32 v66, 0\n\t"
    "v_mov_b32 v67, 0\n\t"
    "v_mov_b32 v68, 0\n\t"
    "v_mov_b32 v69, 0\n\t"
    "v_mov_b32 v70, 0\n\t"
    "v_mov_b32 v71, 0\n\t"
    "v_mov_b32 v72, 0\n\t"
    "v_mov_b32 v73, 0\n\t"
    "v_mov_b32 v74, 0\n\t"
    "v_mov_b32 v75, 0\n\t"
    "v_mov_b32 v76, 0\n\t"
    "v_mov_b32 v77, 0\n\t"
    "v_mov_b32 v78, 0\n\t"
    "v_mov_b32 v79, 0\n\t"
    "v_mov_b32 v164, 1\n\t"
    "v_mov_b32 v84, %[tl]\n\t"
    "v_mov_b32 v85, %[th]\n\t"
    "v_lshlrev_b32 v86, 3, v156\n\t"
    "s_waitcnt vmcnt(0)\n\t"
    "v_lshlrev_b32 v0, 16, v20\n\t"
    "v_and_b32 v1, 0xffff0000, v20\n\t"
    "v_lshlrev_b32 v2, 16, v21\n\t"
    "v_and_b32 v3, 0xffff0000, v21\n\t"
    "v_lshlrev_b32 v4, 16, v22\n\t"
    "v_and_b32 v5, 0xffff0000, v22\n\t"
    "v_lshlrev_b32 v6, 16, v23\n\t"
    "v_and_b32 v7, 0xffff0000, v23\n\t"
    "v_lshlrev_b32 v8, 16, v24\n\t"
    "v_and_b32 v9, 0xffff0000, v24\n\t"
    "v_lshlrev_b32 v10, 16, v25\n\t"
    "v_and_b32 v11, 0xffff0000, v25\n\t"
    "v_lshlrev_b32 v12, 16, v26\n\t"
    "v_and_b32 v13, 0xffff0000, v26\n\t"
    "v_lshlrev_b32 v14, 16, v27\n\t"
    "v_and_b32 v15, 0xffff0000, v27\n\t"
    "v_lshlrev_b32 v16, 16, v28\n\t"
    "v_and_b32 v17, 0xffff0000, v28\n\t"
    "v_lshlrev_b32 v18, 16, v29\n\t"
    "v_and_b32 v19, 0xffff0000, v29\n\t"
    "v_mov_b32 v20, 0\n\t"
    "v_mov_b32 v21, 0\n\t"
    "v_mov_b32 v22, 0\n\t"
    "v_mov_b32 v23, 0\n\t"
    "v_mov_b32 v24, 0\n\t"
    "v_mov_b32 v25, 0\n\t"
    "v_mov_b32 v26, 0\n\t"
    "v_mov_b32 v27, 0\n\t"
    "v_mov_b32 v28, 0\n\t"
    "v_mov_b32 v29, 0\n\t"
    "s_sleep 20\n\t"
    "s_mov_b64 exec, 0x7\n\t"
    "Lp1_%=:\n\t"
    "global_load_dwordx2 v[166:167], v80, %[ws] sc1\n\t"
    "s_waitcnt vmcnt(0)\n\t"
    "v_cmp_ne_u64 vcc, %[tag], v[166:167]\n\t"
    "s_cbranch_vccz Lp1_%=d\n\t"
    "s_sleep 1\n\t"
    "s_sub_u32 %[sp], %[sp], 1\n\t"
    "s_cbranch_scc0 Lp1_%=\n\t"
    "Lp1_%=d:\n\t"
    "s_mov_b64 exec, -1\n\t"
    "s_mov_b64 s[44:45], %[kv]\n\t"
    "s_add_u32 m0, %[mk], 0\n\t"
    "s_nop 0\n\t"
    "global_load_lds_dwordx4 v156, s[44:45] offset:0 sc1\n\t"
    "global_load_lds_dwordx4 v156, s[44:45] offset:1024 sc1\n\t"
    "global_load_lds_dwordx4 v156, s[44:45] offset:2048 sc1\n\t"
    "global_load_lds_dwordx4 v156, s[44:45] offset:3072 sc1\n\t"
    "s_add_u32 s44, s44, 0x1000\n\t"
    "s_addc_u32 s45, s45, 0\n\t"
    "s_add_u32 m0, %[mk], 4096\n\t"
    "s_nop 0\n\t"
    "global_load_lds_dwordx4 v156, s[44:45] sc1\n\t"
    "s_mov_b32 m0, %[mq]\n\t"
    "s_nop 0\n\t"
    "global_load_lds_dwordx4 v156, %[qb] sc1\n\t"
    "global_load_lds_dwordx4 v156, %[qb] offset:1024 sc1\n\t"
    "s_mov_b64 exec, 0xffffffff\n\t"
    "global_load_lds_dwordx4 v156, %[qb] offset:2048 sc1\n\t"
    "s_mov_b64 exec, -1\n\t"
    "s_waitcnt vmcnt(0)\n\t"
    "s_barrier\n\t"
    "s_mov_b64 exec, 0xf\n\t"
    "Lp2_%=:\n\t"
    "global_load_dwordx2 v[166:167], v81, %[ws] sc1\n\t"
    "s_waitcnt vmcnt(0)\n\t"
    "v_cmp_ne_u64 vcc, %[tag], v[166:167]\n\t"
    "s_cbranch_vccz Lp2_%=d\n\t"
    "s_sleep 1\n\t"
    "s_sub_u32 %[sp], %[sp], 1\n\t"
    "s_cbranch_scc0 Lp2_%=\n\t"
    "Lp2_%=d:\n\t"
    "s_mov_b64 exec, -1\n\t"
    "s_add_u32 s44, s44, 0x4000\n\t"
    "s_addc_u32 s45, s45, 0\n\t"
    "s_add_u32 m0, %[mk], 20480\n\t"
    "s_nop 0\n\t"
    "global_load_lds_dwordx4 v156, s[44:45] offset:0 sc1\n\t"
    "global_load_lds_dwordx4 v156, s[44:45] offset:1024 sc1\n\t"
    "global_load_lds_dwordx4 v156, s[44:45] offset:2048 sc1\n\t"
    "global_load_lds_dwordx4 v156, s[44:45] offset:3072 sc1\n\t"
    "s_add_u32 s44, s44, 0x1000\n\t"
    "s_addc_u32 s45, s45, 0\n\t"
    "s_add_u32 m0, %[mk], 24576\n\t"
    "s_nop 0\n\t"
    "global_load_lds_dwordx4 v156, s[44:45] sc1\n\t"
    "s_add_u32 s44, s44, 0x4000\n\t"
    "s_addc_u32 s45, s45, 0\n\t"
    "s_add_u32 m0, %[mk], 40960\n\t"
    "s_nop 0\n\t"
    "global_load_lds_dwordx4 v156, s[44:45] offset:0 sc1\n\t"
    "global_load_lds_dwordx4 v156, s[44:45] offset:1024 sc1\n\t"
    "global_load_lds_dwordx4 v156, s[44:45] offset:2048 sc1\n\t"
    "global_load_lds_dwordx4 v156, s[44:45] offset:3072 sc1\n\t"
    "s_add_u32 s44, s44, 0x1000\n\t"
    "s_addc_u32 s45, s45, 0\n\t"
    "s_add_u32 m0, %[mk], 45056\n\t"
    "s_nop 0\n\t"
    "global_load_lds_dwordx4 v156, s[44:45] sc1\n\t"
    "s_waitcnt vmcnt(5)\n\t"
    "s_mov_b64 exec, 1\n\t"
    "ds_add_u32 v163, v164 offset:52\n\t"
    "s_mov_b64 exec, -1\n\t"
    "s_cmp_eq_u32 %[rto], 0\n\t"
    "s_cbranch_scc1 Lnr_%=\n\t"
    "buffer_store_dwordx4 v[88:91], v156, %[rs], %[rto] offen offset:0 sc1\n\t"
    "buffer_store_dwordx4 v[92:95], v156, %[rs], %[rto] offen offset:1024 sc1\n\t"
    "buffer_store_dwordx4 v[96:99], v156, %[rs], %[rto] offen offset:2048 sc1\n\t"
    "buffer_store_dwordx4 v[100:103], v156, %[rs], %[rto] offen offset:3072 sc1\n\t"
    "s_add_u32 %[t0], %[rto], 0x1000\n\t"
    "buffer_store_dwordx4 v[104:107], v156, %[rs], %[t0] offen sc1\n\t"
    "Lnr_%=:\n\t"
    "s_mov_b64 exec, 0x3ff\n\t"
    "Lp3_%=:\n\t"
    "global_load_dwordx2 v[166:167], v82, %[ws] sc1\n\t"
    "s_waitcnt vmcnt(0)\n\t"
    "v_cmp_ne_u64 vcc, %[tag], v[166:167]\n\t"
    "s_cbranch_vccz Lp3_%=d\n\t"
    "s_sleep 1\n\t"
    "s_sub_u32 %[sp], %[sp], 1\n\t"
    "s_cbranch_scc0 Lp3_%=\n\t"
    "Lp3_%=d:\n\t"
    "s_mov_b64 exec, -1\n\t"
    "s_mov_b64 exec, 1\n\t"
    "ds_add_u32 v163, v164 offset:4\n\t"
    "s_mov_b64 exec, -1\n\t"
    "s_cmp_eq_u32 %[rto], 0\n\t"
    "s_cbranch_scc1 Lnf_%=\n\t"
    "s_mov_b64 exec, 1\n\t"
    "buffer_store_dwordx2 v[84:85], v86, %[rs], %[rfo] offen sc1\n\t"
    "s_mov_b64 exec, -1\n\t"
    "Lnf_%=:\n\t"
    "Lq_%=:\n\t"
    "ds_read_b32 v166, v163 offset:48\n\t"
    "s_waitcnt lgkmcnt(0)\n\t"
    "v_readfirstlane_b32 %[t0], v166\n\t"
    "s_cmp_ge_u32 %[t0], 8\n\t"
    "s_cbranch_scc1 Lq_%=d\n\t"
    "s_sleep 2\n\t"
    "s_sub_u32 %[sp], %[sp], 1\n\t"
    "s_cbranch_scc0 Lq_%=\n\t"
    "Lq_%=d:\n\t"
    "s_cmp_eq_u32 %[pub], 0\n\t"
    "s_cbranch_scc1 Lnp_%=\n\t"
    "s_mov_b64 exec, 1\n\t"
    "buffer_store_dwordx2 v[84:85], v86, %[rs], %[pub] offen sc1\n\t"
    "s_mov_b64 exec, -1\n\t"
    "Lnp_%=:\n\t"
    "s_mov_b64 exec, 0xfffffff\n\t"
    "Lp4_%=:\n\t"
    "global_load_dwordx2 v[166:167], v83, %[ws] sc1\n\t"
    "s_waitcnt vmcnt(0)\n\t"
    "v_cmp_ne_u64 vcc, %[tag], v[166:167]\n\t"
    "s_cbranch_vccz Lp4_%=d\n\t"
    "s_sleep 4\n\t"
    "s_sub_u32 %[sp], %[sp], 1\n\t"
    "s_cbranch_scc0 Lp4_%=\n\t"
    "Lp4_%=d:\n\t"
    "s_mov_b64 exec, -1\n\t"
    "Lr_%=:\n\t"
    "ds_read_b32 v166, v163 offset:8\n\t"
    "s_waitcnt lgkmcnt(0)\n\t"
    "v_readfirstlane_b32 %[t0], v166\n\t"
    "s_cmp_ge_u32 %[t0], 4\n\t"
    "s_cbranch_scc1 Lr_%=d\n\t"
    "s_sleep 2\n\t"
    "s_sub_u32 %[sp], %[sp], 1\n\t"
    "s_cbranch_scc0 Lr_%=\n\t"
    "Lr_%=d:\n\t"
    "s_mov_b64 s[44:45], %[wt]\n\t"
    "s_add_u32 m0, %[mw], 0\n\t"
    "s_nop 0\n\t"
    "global_load_lds_dwordx4 v156, s[44:45] offset:0 sc1\n\t"
    "global_load_lds_dwordx4 v156, s[44:45] offset:1024 sc1\n\t"
    "global_load_lds_dwordx4 v156, s[44:45] offset:2048 sc1\n\t"
    "global_load_lds_dwordx4 v156, s[44:45] offset:3072 sc1\n\t"
    "s_add_u32 s44, s44, 0x1000\n\t"
    "s_addc_u32 s45, s45, 0\n\t"
    "s_add_u32 m0, %[mw], 4096\n\t"
    "s_nop 0\n\t"
    "global_load_lds_dwordx4 v156, s[44:45] sc1\n\t"
    "s_add_u32 s44, s44, 0x27000\n\t"
    "s_addc_u32 s45, s45, 0\n\t"
    "s_add_u32 m0, %[mw], 20480\n\t"
    "s_nop 0\n\t"
    "global_load_lds_dwordx4 v156, s[44:45] offset:0 sc1\n\t"
    "global_load_lds_dwordx4 v156, s[44:45] offset:1024 sc1\n\t"
    "global_load_lds_dwordx4 v156, s[44:45] offset:2048 sc1\n\t"
    "global_load_lds_dwordx4 v156, s[44:45] offset:3072 sc1\n\t"
    "s_add_u32 s44, s44, 0x1000\n\t"
    "s_addc_u32 s45, s45, 0\n\t"
    "s_add_u32 m0, %[mw], 24576\n\t"
    "s_nop 0\n\t"
    "global_load_lds_dwordx4 v156, s[44:45] sc1\n\t"
    "s_add_u32 s44, s44, 0x27000\n\t"
    "s_addc_u32 s45, s45, 0\n\t"
    "s_add_u32 m0, %[mw], 102400\n\t"
    "s_nop 0\n\t"
    "global_load_lds_dwordx4 v156, s[44:45] offset:0 sc1\n\t"
    "global_load_lds_dwordx4 v156, s[44:45] offset:1024 sc1\n\t"
    "global_load_lds_dwordx4 v156, s[44:45] offset:2048 sc1\n\t"
    "global_load_lds_dwordx4 v156, s[44:45] offset:3072 sc1\n\t"
    "s_add_u32 s44, s44, 0x1000\n\t"
    "s_addc_u32 s45, s45, 0\n\t"
    "s_add_u32 m0, %[mw], 106496\n\t"
    "s_nop 0\n\t"
    "global_load_lds_dwordx4 v156, s[44:45] sc1\n\t"
    "s_add_u32 s44, s44, 0x27000\n\t"
    "s_addc_u32 s45, s45, 0\n\t"
    "s_add_u32 m0, %[mw], 122880\n\t"
    "s_nop 0\n\t"
    "global_load_lds_dwordx4 v156, s[44:45] offset:0 sc1\n\t"
    "global_load_lds_dwordx4 v156, s[44:45] offset:1024 sc1\n\t"
    "global_load_lds_dwordx4 v156, s[44:45] offset:2048 sc1\n\t"
    "global_load_lds_dwordx4 v156, s[44:45] offset:3072 sc1\n\t"
    "s_add_u32 s44, s44, 0x1000\n\t"
    "s_addc_u32 s45, s45, 0\n\t"
    "s_add_u32 m0, %[mw], 126976\n\t"
    "s_nop 0\n\t"
    "global_load_lds_dwordx4 v156, s[44:45] sc1\n\t"
    "s_add_u32 s44, s44, 0x27000\n\t"
    "s_addc_u32 s45, s45, 0\n\t"
    "s_cmp_eq_u32 %[mw], 15360\n\t"
    "s_cbranch_scc1 Lw4_%=\n\t"
    "s_add_u32 m0, %[mw], 143360\n\t"
    "s_nop 0\n\t"
    "global_load_lds_dwordx4 v156, s[44:45] offset:0 sc1\n\t"
    "global_load_lds_dwordx4 v156, s[44:45] offset:1024 sc1\n\t"
    "global_load_lds_dwordx4 v156, s[44:45] offset:2048 sc1\n\t"
    "global_load_lds_dwordx4 v156, s[44:45] offset:3072 sc1\n\t"
    "s_add_u32 s44, s44, 0x1000\n\t"
    "s_addc_u32 s45, s45, 0\n\t"
    "s_add_u32 m0, %[mw], 147456\n\t"
    "s_nop 0\n\t"
    "global_load_lds_dwordx4 v156, s[44:45] sc1\n\t"
    "s_add_u32 s44, s44, 0x27000\n\t"
    "s_addc_u32 s45, s45, 0\n\t"
    "Lw4_%=:\n\t"
    "s_mov_b64 s[44:45], %[hb]\n\t"
    "s_mov_b64 s[46:47], %[wb]\n\t"
    "global_load_dwordx4 v[80:83], v152, s[44:45] sc1\n\t"
    "global_load_dwordx4 v[84:87], v153, s[44:45] sc1\n\t"
    "global_load_dwordx4 v[88:91], v154, s[44:45] sc1\n\t"
    "global_load_dwordx4 v[92:95], v155, s[44:45] sc1\n\t"
    "global_load_dwordx4 v[96:99], v156, s[46:47] offset:-2048 sc1\n\t"
    "global_load_dwordx4 v[100:103], v156, s[46:47] offset:-1024 sc1\n\t"
    "global_load_dwordx4 v[104:107], v156, s[46:47] offset:0 sc1\n\t"
    "global_load_dwordx4 v[108:111], v156, s[46:47] offset:1024 sc1\n\t"
    "global_load_dwordx4 v[112:115], v156, s[46:47] offset:2048 sc1\n\t"
    "s_add_u32 s44, s44, 0x400\n\t"
    "s_addc_u32 s45, s45, 0\n\t"
    "s_add_u32 s46, s46, 0xa000\n\t"
    "s_addc_u32 s47, s47, 0\n\t"
    "global_load_dwordx4 v[116:119], v152, s[44:45] sc1\n\t"
    "global_load_dwordx4 v[120:123], v153, s[44:45] sc1\n\t"
    "global_load_dwordx4 v[124:127], v154, s[44:45] sc1\n\t"
    "global_load_dwordx4 v[128:131], v155, s[44:45] sc1\n\t"
    "global_load_dwordx4 v[132:135], v156, s[46:47] offset:-2048 sc1\n\t"
    "global_load_dwordx4 v[136:139], v156, s[46:47] offset:-1024 sc1\n\t"
    "global_load_dwordx4 v[140:143], v156, s[46:47] offset:0 sc1\n\t"
    "global_load_dwordx4 v[144:147], v156, s[46:47] offset:1024 sc1\n\t"
    "global_load_dwordx4 v[148:151], v156, s[46:47] offset:2048 sc1\n\t"
    "s_add_u32 s44, s44, 0x400\n\t"
    "s_addc_u32 s45, s45, 0\n\t"
    "s_add_u32 s46, s46, 0xa000\n\t"
    "s_addc_u32 s47, s47, 0\n\t"
    "s_mov_b32 %[cnt], 5\n\t"
    "Lloop_%=:\n\t"
    "s_waitcnt vmcnt(9)\n\t"
    "v_mfma_f32_16x16x32_bf16 v[0:3], v[96:99], v[80:83], v[0:3]\n\t"
    "s_nop 11\n\t"
    "v_mfma_f32_16x16x32_bf16 v[20:23], v[96:99], v[84:87], v[20:23]\n\t"
    "s_nop 11\n\t"
    "v_mfma_f32_16x16x32_bf16 v[40:43], v[96:99], v[88:91], v[40:43]\n\t"
    "s_nop 11\n\t"
    "v_mfma_f32_16x16x32_bf16 v[60:63], v[96:99], v[92:95], v[60:63]\n\t"
    "s_nop 11\n\t"
    "v_mfma_f32_16x16x32_bf16 v[64:67], v[100:103], v[92:95], v[64:67]\n\t"
    "s_nop 11\n\t"
    "v_mfma_f32_16x16x32_bf16 v[44:47], v[100:103], v[88:91], v[44:47]\n\t"
    "s_nop 11\n\t"
    "v_mfma_f32_16x16x32_bf16 v[24:27], v[100:103], v[84:87], v[24:27]\n\t"
    "s_nop 11\n\t"
    "v_mfma_f32_16x16x32_bf16 v[4:7], v[100:103], v[80:83], v[4:7]\n\t"
    "s_nop 11\n\t"
    "v_mfma_f32_16x16x32_bf16 v[8:11], v[104:107], v[80:83], v[8:11]\n\t"
    "s_nop 11\n\t"
    "v_mfma_f32_16x16x32_bf16 v[28:31], v[104:107], v[84:87], v[28:31]\n\t"
    "s_nop 11\n\t"
    "v_mfma_f32_16x16x32_bf16 v[48:51], v[104:107], v[88:91], v[48:51]\n\t"
    "s_nop 11\n\t"
    "v_mfma_f32_16x16x32_bf16 v[68:71], v[104:107], v[92:95], v[68:71]\n\t"
    "s_nop 11\n\t"
    "v_mfma_f32_16x16x32_bf16 v[72:75], v[108:111], v[92:95], v[72:75]\n\t"
    "s_nop 11\n\t"
    "v_mfma_f32_16x16x32_bf16 v[52:55], v[108:111], v[88:91], v[52:55]\n\t"
    "s_nop 11\n\t"
    "v_mfma_f32_16x16x32_bf16 v[32:35], v[108:111], v[84:87], v[32:35]\n\t"
    "s_nop 11\n\t"
    "v_mfma_f32_16x16x32_bf16 v[12:15], v[108:111], v[80:83], v[12:15]\n\t"
    "s_nop 11\n\t"
    "v_mfma_f32_16x16x32_bf16 v[16:19], v[112:115], v[80:83], v[16:19]\n\t"
    "s_nop 11\n\t"
    "v_mfma_f32_16x16x32_bf16 v[36:39], v[112:115], v[84:87], v[36:39]\n\t"
    "s_nop 11\n\t"
    "v_mfma_f32_16x16x32_bf16 v[56:59], v[112:115], v[88:91], v[56:59]\n\t"
    "s_nop 11\n\t"
    "v_mfma_f32_16x16x32_bf16 v[76:79], v[112:115], v[92:95], v[76:79]\n\t"
    "s_nop 11\n\t"
    "global_load_dwordx4 v[80:83], v152, s[44:45] sc1\n\t"
    "global_load_dwordx4 v[84:87], v153, s[44:45] sc1\n\t"
    "global_load_dwordx4 v[88:91], v154, s[44:45] sc1\n\t"
    "global_load_dwordx4 v[92:95], v155, s[44:45] sc1\n\t"
    "global_load_dwordx4 v[96:99], v156, s[46:47] offset:-2048 sc1\n\t"
    "global_load_dwordx4 v[100:103], v156, s[46:47] offset:-1024 sc1\n\t"
    "global_load_dwordx4 v[104:107], v156, s[46:47] offset:0 sc1\n\t"
    "global_load_dwordx4 v[108:111], v156, s[46:47] offset:1024 sc1\n\t"
    "global_load_dwordx4 v[112:115], v156, s[46:47] offset:2048 sc1\n\t"
    "s_add_u32 s44, s44, 0x400\n\t"
    "s_addc_u32 s45, s45, 0\n\t"
    "s_add_u32 s46, s46, 0xa000\n\t"
    "s_addc_u32 s47, s47, 0\n\t"
    "s_waitcnt vmcnt(9)\n\t"
    "v_mfma_f32_16x16x32_bf16 v[0:3], v[132:135], v[116:119], v[0:3]\n\t"
    "s_nop 11\n\t"
    "v_mfma_f32_16x16x32_bf16 v[20:23], v[132:135], v[120:123], v[20:23]\n\t"
    "s_nop 11\n\t"
    "v_mfma_f32_16x16x32_bf16 v[40:43], v[132:135], v[124:127], v[40:43]\n\t"
    "s_nop 11\n\t"
    "v_mfma_f32_16x16x32_bf16 v[60:63], v[132:135], v[128:131], v[60:63]\n\t"
    "s_nop 11\n\t"
    "v_mfma_f32_16x16x32_bf16 v[64:67], v[136:139], v[128:131], v[64:67]\n\t"
    "s_nop 11\n\t"
    "v_mfma_f32_16x16x32_bf16 v[44:47], v[136:139], v[124:127], v[44:47]\n\t"
    "s_nop 11\n\t"
    "v_mfma_f32_16x16x32_bf16 v[24:27], v[136:139], v[120:123], v[24:27]\n\t"
    "s_nop 11\n\t"
    "v_mfma_f32_16x16x32_bf16 v[4:7], v[136:139], v[116:119], v[4:7]\n\t"
    "s_nop 11\n\t"
    "v_mfma_f32_16x16x32_bf16 v[8:11], v[140:143], v[116:119], v[8:11]\n\t"
    "s_nop 11\n\t"
    "v_mfma_f32_16x16x32_bf16 v[28:31], v[140:143], v[120:123], v[28:31]\n\t"
    "s_nop 11\n\t"
    "v_mfma_f32_16x16x32_bf16 v[48:51], v[140:143], v[124:127], v[48:51]\n\t"
    "s_nop 11\n\t"
    "v_mfma_f32_16x16x32_bf16 v[68:71], v[140:143], v[128:131], v[68:71]\n\t"
    "s_nop 11\n\t"
    "v_mfma_f32_16x16x32_bf16 v[72:75], v[144:147], v[128:131], v[72:75]\n\t"
    "s_nop 11\n\t"
    "v_mfma_f32_16x16x32_bf16 v[52:55], v[144:147], v[124:127], v[52:55]\n\t"
    "s_nop 11\n\t"
    "v_mfma_f32_16x16x32_bf16 v[32:35], v[144:147], v[120:123], v[32:35]\n\t"
    "s_nop 11\n\t"
    "v_mfma_f32_16x16x32_bf16 v[12:15], v[144:147], v[116:119], v[12:15]\n\t"
    "s_nop 11\n\t"
    "v_mfma_f32_16x16x32_bf16 v[16:19], v[148:151], v[116:119], v[16:19]\n\t"
    "s_nop 11\n\t"
    "v_mfma_f32_16x16x32_bf16 v[36:39], v[148:151], v[120:123], v[36:39]\n\t"
    "s_nop 11\n\t"
    "v_mfma_f32_16x16x32_bf16 v[56:59], v[148:151], v[124:127], v[56:59]\n\t"
    "s_nop 11\n\t"
    "v_mfma_f32_16x16x32_bf16 v[76:79], v[148:151], v[128:131], v[76:79]\n\t"
    "s_nop 11\n\t"
    "global_load_dwordx4 v[116:119], v152, s[44:45] sc1\n\t"
    "global_load_dwordx4 v[120:123], v153, s[44:45] sc1\n\t"
    "global_load_dwordx4 v[124:127], v154, s[44:45] sc1\n\t"
    "global_load_dwordx4 v[128:131], v155, s[44:45] sc1\n\t"
    "global_load_dwordx4 v[132:135], v156, s[46:47] offset:-2048 sc1\n\t"
    "global_load_dwordx4 v[136:139], v156, s[46:47] offset:-1024 sc1\n\t"
    "global_load_dwordx4 v[140:143], v156, s[46:47] offset:0 sc1\n\t"
    "global_load_dwordx4 v[144:147], v156, s[46:47] offset:1024 sc1\n\t"
    "global_load_dwordx4 v[148:151], v156, s[46:47] offset:2048 sc1\n\t"
    "s_add_u32 s44, s44, 0x400\n\t"
    "s_addc_u32 s45, s45, 0\n\t"
    "s_add_u32 s46, s46, 0xa000\n\t"
    "s_addc_u32 s47, s47, 0\n\t"
    "s_sub_u32 %[cnt], %[cnt], 1\n\t"
    "s_cmp_lg_u32 %[cnt], 0\n\t"
    "s_cbranch_scc1 Lloop_%=\n\t"
    "La_%=:\n\t"
    "ds_read_b32 v166, v163 offset:12\n\t"
    "s_waitcnt lgkmcnt(0)\n\t"
    "v_readfirstlane_b32 %[t0], v166\n\t"
    "s_cmp_ge_u32 %[t0], 8\n\t"
    "s_cbranch_scc1 La_%=d\n\t"
    "s_sleep 2\n\t"
    "s_sub_u32 %[sp], %[sp], 1\n\t"
    "s_cbranch_scc0 La_%=\n\t"
    "La_%=d:\n\t"
    "s_waitcnt vmcnt(9)\n\t"
    "v_mfma_f32_16x16x32_bf16 v[0:3], v[96:99], v[80:83], v[0:3]\n\t"
    "v_mfma_f32_16x16x32_bf16 v[20:23], v[96:99], v[84:87], v[20:23]\n\t"
    "v_mfma_f32_16x16x32_bf16 v[40:43], v[96:99], v[88:91], v[40:43]\n\t"
    "v_mfma_f32_16x16x32_bf16 v[60:63], v[96:99], v[92:95], v[60:63]\n\t"
    "v_mfma_f32_16x16x32_bf16 v[64:67], v[100:103], v[92:95], v[64:67]\n\t"
    "v_mfma_f32_16x16x32_bf16 v[44:47], v[100:103], v[88:91], v[44:47]\n\t"
    "v_mfma_f32_16x16x32_bf16 v[24:27], v[100:103], v[84:87], v[24:27]\n\t"
    "v_mfma_f32_16x16x32_bf16 v[4:7], v[100:103], v[80:83], v[4:7]\n\t"
    "v_mfma_f32_16x16x32_bf16 v[8:11], v[104:107], v[80:83], v[8:11]\n\t"
    "v_mfma_f32_16x16x32_bf16 v[28:31], v[104:107], v[84:87], v[28:31]\n\t"
    "v_mfma_f32_16x16x32_bf16 v[48:51], v[104:107], v[88:91], v[48:51]\n\t"
    "v_mfma_f32_16x16x32_bf16 v[68:71], v[104:107], v[92:95], v[68:71]\n\t"
    "v_mfma_f32_16x16x32_bf16 v[72:75], v[108:111], v[92:95], v[72:75]\n\t"
    "v_mfma_f32_16x16x32_bf16 v[52:55], v[108:111], v[88:91], v[52:55]\n\t"
    "v_mfma_f32_16x16x32_bf16 v[32:35], v[108:111], v[84:87], v[32:35]\n\t"
    "v_mfma_f32_16x16x32_bf16 v[12:15], v[108:111], v[80:83], v[12:15]\n\t"
    "v_mfma_f32_16x16x32_bf16 v[16:19], v[112:115], v[80:83], v[16:19]\n\t"
    "v_mfma_f32_16x16x32_bf16 v[36:39], v[112:115], v[84:87], v[36:39]\n\t"
    "v_mfma_f32_16x16x32_bf16 v[56:59], v[112:115], v[88:91], v[56:59]\n\t"
    "v_mfma_f32_16x16x32_bf16 v[76:79], v[112:115], v[92:95], v[76:79]\n\t"
    "s_waitcnt vmcnt(0)\n\t"
    "v_mfma_f32_16x16x32_bf16 v[20:23], v[132:135], v[120:123], v[20:23]\n\t"
    "v_mfma_f32_16x16x32_bf16 v[24:27], v[136:139], v[120:123], v[24:27]\n\t"
    "v_mfma_f32_16x16x32_bf16 v[28:31], v[140:143], v[120:123], v[28:31]\n\t"
    "v_mfma_f32_16x16x32_bf16 v[32:35], v[144:147], v[120:123], v[32:35]\n\t"
    "v_mfma_f32_16x16x32_bf16 v[36:39], v[148:151], v[120:123], v[36:39]\n\t"
    "v_mfma_f32_16x16x32_bf16 v[40:43], v[132:135], v[124:127], v[40:43]\n\t"
    "v_mfma_f32_16x16x32_bf16 v[44:47], v[136:139], v[124:127], v[44:47]\n\t"
    "v_mfma_f32_16x16x32_bf16 v[48:51], v[140:143], v[124:127], v[48:51]\n\t"
    "v_mfma_f32_16x16x32_bf16 v[52:55], v[144:147], v[124:127], v[52:55]\n\t"
    "v_mfma_f32_16x16x32_bf16 v[56:59], v[148:151], v[124:127], v[56:59]\n\t"
    "v_mfma_f32_16x16x32_bf16 v[60:63], v[132:135], v[128:131], v[60:63]\n\t"
    "v_mfma_f32_16x16x32_bf16 v[64:67], v[136:139], v[128:131], v[64:67]\n\t"
    "v_mfma_f32_16x16x32_bf16 v[68:71], v[140:143], v[128:131], v[68:71]\n\t"
    "v_mfma_f32_16x16x32_bf16 v[72:75], v[144:147], v[128:131], v[72:75]\n\t"
    "v_mfma_f32_16x16x32_bf16 v[76:79], v[148:151], v[128:131], v[76:79]\n\t"
    "v_mfma_f32_16x16x32_bf16 v[0:3], v[132:135], v[116:119], v[0:3]\n\t"
    "v_mfma_f32_16x16x32_bf16 v[4:7], v[136:139], v[116:119], v[4:7]\n\t"
    "v_mfma_f32_16x16x32_bf16 v[8:11], v[140:143], v[116:119], v[8:11]\n\t"
    "v_mfma_f32_16x16x32_bf16 v[12:15], v[144:147], v[116:119], v[12:15]\n\t"
    "v_mfma_f32_16x16x32_bf16 v[16:19], v[148:151], v[116:119], v[16:19]\n\t"
    "global_load_dwordx4 v[100:103], v156, %[w0] offset:-2048 sc1\n\t"
    "global_load_dwordx4 v[104:107], v156, %[w0] offset:-1024 sc1\n\t"
    "global_load_dwordx4 v[108:111], v156, %[w0] offset:0 sc1\n\t"
    "global_load_dwordx4 v[112:115], v156, %[w0] offset:1024 sc1\n\t"
    "global_load_dwordx4 v[116:119], v156, %[w0] offset:2048 sc1\n\t"
    "v_add_u32 v81, %[r1], v156\n\t"
    "v_add_u32 v82, %[r2], v156\n\t"
    "v_add_u32 v83, %[r3], v156\n\t"
    "v_add_u32 v80, %[r0], v156\n\t"
    "ds_write_b128 v81, v[20:23] offset:0\n\t"
    "ds_write_b128 v81, v[24:27] offset:1024\n\t"
    "ds_write_b128 v81, v[28:31] offset:2048\n\t"
    "ds_write_b128 v81, v[32:35] offset:3072\n\t"
    "ds_write_b128 v81, v[36:39] offset:4096\n\t"
    "ds_write_b128 v82, v[40:43] offset:0\n\t"
    "ds_write_b128 v82, v[44:47] offset:1024\n\t"
    "ds_write_b128 v82, v[48:51] offset:2048\n\t"
    "ds_write_b128 v82, v[52:55] offset:3072\n\t"
    "ds_write_b128 v82, v[56:59] offset:4096\n\t"
    "ds_write_b128 v83, v[60:63] offset:0\n\t"
    "ds_write_b128 v83, v[64:67] offset:1024\n\t"
    "ds_write_b128 v83, v[68:71] offset:2048\n\t"
    "ds_write_b128 v83, v[72:75] offset:3072\n\t"
    "ds_write_b128 v83, v[76:79] offset:4096\n\t"
    "s_waitcnt lgkmcnt(0)\n\t"
    "s_mov_b64 exec, 1\n\t"
    "ds_add_u32 v163, v164 offset:8\n\t"
    "s_mov_b64 exec, -1\n\t"
    "Lb_%=:\n\t"
    "ds_read_b32 v166, v163 offset:8\n\t"
    "s_waitcnt lgkmcnt(0)\n\t"
    "v_readfirstlane_b32 %[t0], v166\n\t"
    "s_cmp_ge_u32 %[t0], 8\n\t"
    "s_cbranch_scc1 Lb_%=d\n\t"
    "s_sleep 1\n\t"
    "s_sub_u32 %[sp], %[sp], 1\n\t"
    "s_cbranch_scc0 Lb_%=\n\t"
    "Lb_%=d:\n\t"
    "ds_read_b128 v[20:23], v80 offset:0\n\t"
    "ds_read_b128 v[24:27], v80 offset:1024\n\t"
    "ds_read_b128 v[28:31], v80 offset:2048\n\t"
    "ds_read_b128 v[32:35], v80 offset:3072\n\t"
    "ds_read_b128 v[36:39], v80 offset:4096\n\t"
    "ds_read_b128 v[40:43], v80 offset:5120\n\t"
    "ds_read_b128 v[44:47], v80 offset:6144\n\t"
    "ds_read_b128 v[48:51], v80 offset:7168\n\t"
    "ds_read_b128 v[52:55], v80 offset:8192\n\t"
    "ds_read_b128 v[56:59], v80 offset:9216\n\t"
    "ds_read_b128 v[60:63], v80 offset:10240\n\t"
    "ds_read_b128 v[64:67], v80 offset:11264\n\t"
    "ds_read_b128 v[68:71], v80 offset:12288\n\t"
    "ds_read_b128 v[72:75], v80 offset:13312\n\t"
    "ds_read_b128 v[76:79], v80 offset:14336\n\t"
    "s_waitcnt lgkmcnt(10)\n\t"
    "v_pk_add_f32 v[0:1], v[0:1], v[20:21]\n\t"
    "v_pk_add_f32 v[2:3], v[2:3], v[22:23]\n\t"
    "v_pk_add_f32 v[4:5], v[4:5], v[24:25]\n\t"
    "v_pk_add_f32 v[6:7], v[6:7], v[26:27]\n\t"
    "v_pk_add_f32 v[8:9], v[8:9], v[28:29]\n\t"
    "v_pk_add_f32 v[10:11], v[10:11], v[30:31]\n\t"
    "v_pk_add_f32 v[12:13], v[12:13], v[32:33]\n\t"
    "v_pk_add_f32 v[14:15], v[14:15], v[34:35]\n\t"
    "v_pk_add_f32 v[16:17], v[16:17], v[36:37]\n\t"
    "v_pk_add_f32 v[18:19], v[18:19], v[38:39]\n\t"
    "s_waitcnt lgkmcnt(5)\n\t"
    "v_pk_add_f32 v[0:1], v[0:1], v[40:41]\n\t"
    "v_pk_add_f32 v[2:3], v[2:3], v[42:43]\n\t"
    "v_pk_add_f32 v[4:5], v[4:5], v[44:45]\n\t"
    "v_pk_add_f32 v[6:7], v[6:7], v[46:47]\n\t"
    "v_pk_add_f32 v[8:9], v[8:9], v[48:49]\n\t"
    "v_pk_add_f32 v[10:11], v[10:11], v[50:51]\n\t"
    "v_pk_add_f32 v[12:13], v[12:13], v[52:53]\n\t"
    "v_pk_add_f32 v[14:15], v[14:15], v[54:55]\n\t"
    "v_pk_add_f32 v[16:17], v[16:17], v[56:57]\n\t"
    "v_pk_add_f32 v[18:19], v[18:19], v[58:59]\n\t"
    "s_waitcnt lgkmcnt(0)\n\t"
    "v_pk_add_f32 v[0:1], v[0:1], v[60:61]\n\t"
    "v_pk_add_f32 v[2:3], v[2:3], v[62:63]\n\t"
    "v_pk_add_f32 v[4:5], v[4:5], v[64:65]\n\t"
    "v_pk_add_f32 v[6:7], v[6:7], v[66:67]\n\t"
    "v_pk_add_f32 v[8:9], v[8:9], v[68:69]\n\t"
    "v_pk_add_f32 v[10:11], v[10:11], v[70:71]\n\t"
    "v_pk_add_f32 v[12:13], v[12:13], v[72:73]\n\t"
    "v_pk_add_f32 v[14:15], v[14:15], v[74:75]\n\t"
    "v_pk_add_f32 v[16:17], v[16:17], v[76:77]\n\t"
    "v_pk_add_f32 v[18:19], v[18:19], v[78:79]\n\t"
    "ds_read_b128 v[120:123], v160 offset:0\n\t"
    "ds_read_b128 v[124:127], v160 offset:1024\n\t"
    "ds_read_b128 v[128:131], v160 offset:2048\n\t"
    "ds_read_b128 v[132:135], v160 offset:3072\n\t"
    "ds_read_b128 v[136:139], v160 offset:4096\n\t"
    "s_mov_b64 exec, 0xffffffff\n\t"
    "Lf_%=:\n\t"
    "global_load_dwordx2 v[166:167], v162, %[ws] sc1\n\t"
    "s_waitcnt vmcnt(0)\n\t"
    "v_cmp_ne_u64 vcc, %[tag], v[166:167]\n\t"
    "s_cbranch_vccz Lf_%=d\n\t"
    "s_sleep 1\n\t"
    "s_sub_u32 %[sp], %[sp], 1\n\t"
    "s_cbranch_scc0 Lf_%=\n\t"
    "Lf_%=d:\n\t"
    "s_mov_b64 exec, -1\n\t"
    "s_cmp_eq_u32 %[epo], 0\n\t"
    "s_cbranch_scc1 Lne_%=\n\t"
    "v_mov_b32 v20, %[tl]\n\t"
    "v_mov_b32 v21, %[th]\n\t"
    "v_mov_b32 v22, 0\n\t"
    "s_mov_b64 exec, 1\n\t"
    "buffer_store_dwordx2 v[20:21], v22, %[rs], %[epo] offen sc1\n\t"
    "s_waitcnt vmcnt(0)\n\t"
    "s_mov_b64 exec, -1\n\t"
    "Lne_%=:\n\t"
    "s_mov_b64 s[44:45], %[ob]\n\t"
    "s_mov_b64 s[46:47], %[tb]\n\t"
    "v_mov_b32 v165, v159\n\t"
    "v_bfe_u32 v164, v156, 8, 1\n\t"
    "v_mul_u32_u24 v164, 24, v164\n\t"
    "v_add_u32 v164, v164, v159\n\t"
    "global_load_dwordx4 v[20:23], v156, s[44:45] offset:-2048 sc1\n\t"
    "global_load_dwordx4 v[24:27], v156, s[44:45] offset:-1024 sc1\n\t"
    "global_load_dwordx4 v[28:31], v156, s[44:45] offset:0 sc1\n\t"
    "global_load_dwordx4 v[32:35], v156, s[44:45] offset:1024 sc1\n\t"
    "s_add_u32 s44, s44, 0x1000\n\t"
    "s_addc_u32 s45, s45, 0\n\t"
    "global_load_dwordx4 v[36:39], v156, s[44:45] offset:-2048 sc1\n\t"
    "global_load_dwordx4 v[40:43], v156, s[44:45] offset:-1024 sc1\n\t"
    "global_load_dwordx4 v[44:47], v156, s[44:45] offset:0 sc1\n\t"
    "global_load_dwordx4 v[48:51], v156, s[44:45] offset:1024 sc1\n\t"
    "s_add_u32 s44, s44, 0x1000\n\t"
    "s_addc_u32 s45, s45, 0\n\t"
    "global_load_dwordx4 v[52:55], v156, s[44:45] offset:-2048 sc1\n\t"
    "global_load_dwordx4 v[56:59], v156, s[44:45] offset:-1024 sc1\n\t"
    "global_load_dwordx4 v[60:63], v156, s[44:45] offset:0 sc1\n\t"
    "global_load_dwordx4 v[64:67], v156, s[44:45] offset:1024 sc1\n\t"
    "s_add_u32 s44, s44, 0x1000\n\t"
    "s_addc_u32 s45, s45, 0\n\t"
    "global_load_dwordx4 v[68:71], v156, s[44:45] offset:-2048 sc1\n\t"
    "global_load_dwordx4 v[72:75], v156, s[44:45] offset:-1024 sc1\n\t"
    "global_load_dwordx4 v[76:79], v156, s[44:45] offset:0 sc1\n\t"
    "global_load_dwordx4 v[80:83], v156, s[44:45] offset:1024 sc1\n\t"
    "s_add_u32 s44, s44, 0x1000\n\t"
    "s_addc_u32 s45, s45, 0\n\t"
    "global_load_dwordx2 v[84:85], v157, s[46:47] sc1\n\t"
    "global_load_dwordx2 v[86:87], v158, s[46:47] sc1\n\t"
    "s_add_u32 s46, s46, 0x20000\n\t"
    "s_addc_u32 s47, s47, 0\n\t"
    "global_load_dwordx2 v[88:89], v157, s[46:47] sc1\n\t"
    "global_load_dwordx2 v[90:91], v158, s[46:47] sc1\n\t"
    "s_add_u32 s46, s46, 0x20000\n\t"
    "s_addc_u32 s47, s47, 0\n\t"
    "global_load_dwordx2 v[92:93], v157, s[46:47] sc1\n\t"
    "global_load_dwordx2 v[94:95], v158, s[46:47] sc1\n\t"
    "s_add_u32 s46, s46, 0x20000\n\t"
    "s_addc_u32 s47, s47, 0\n\t"
    "global_load_dwordx2 v[96:97], v157, s[46:47] sc1\n\t"
    "global_load_dwordx2 v[98:99], v158, s[46:47] sc1\n\t"
    "s_add_u32 s46, s46, 0x20000\n\t"
    "s_addc_u32 s47, s47, 0\n\t"
    "s_waitcnt vmcnt(23) lgkmcnt(5)\n\t"
    "v_mfma_f32_16x16x32_bf16 v[0:3], v[100:103], v[20:23], v[0:3]\n\t"
    "ds_read_b128 v[140:143], v160 offset:5120\n\t"
    "v_mfma_f32_16x16x32_bf16 v[4:7], v[104:107], v[20:23], v[4:7]\n\t"
    "ds_read_b128 v[144:147], v160 offset:6144\n\t"
    "v_mfma_f32_16x16x32_bf16 v[8:11], v[108:111], v[20:23], v[8:11]\n\t"
    "ds_read_b128 v[148:151], v160 offset:7168\n\t"
    "v_mfma_f32_16x16x32_bf16 v[12:15], v[112:115], v[20:23], v[12:15]\n\t"
    "ds_read_b128 v[152:155], v160 offset:8192\n\t"
    "v_mfma_f32_16x16x32_bf16 v[16:19], v[116:119], v[20:23], v[16:19]\n\t"
    "ds_read_b128 v[156:159], v160 offset:9216\n\t"
    "s_waitcnt vmcnt(22) lgkmcnt(5)\n\t"
    "v_mfma_f32_16x16x32_bf16 v[0:3], v[120:123], v[24:27], v[0:3]\n\t"
    "ds_read_b128 v[100:103], v160 offset:10240\n\t"
    "v_mfma_f32_16x16x32_bf16 v[4:7], v[124:127], v[24:27], v[4:7]\n\t"
    "ds_read_b128 v[104:107], v160 offset:11264\n\t"
    "v_mfma_f32_16x16x32_bf16 v[8:11], v[128:131], v[24:27], v[8:11]\n\t"
    "ds_read_b128 v[108:111], v160 offset:12288\n\t"
    "v_mfma_f32_16x16x32_bf16 v[12:15], v[132:135], v[24:27], v[12:15]\n\t"
    "ds_read_b128 v[112:115], v160 offset:13312\n\t"
    "v_mfma_f32_16x16x32_bf16 v[16:19], v[136:139], v[24:27], v[16:19]\n\t"
    "ds_read_b128 v[116:119], v160 offset:14336\n\t"
    "s_waitcnt vmcnt(21) lgkmcnt(5)\n\t"
    "v_mfma_f32_16x16x32_bf16 v[0:3], v[140:143], v[28:31], v[0:3]\n\t"
    "ds_read_b128 v[120:123], v160 offset:15360\n\t"
    "v_mfma_f32_16x16x32_bf16 v[4:7], v[144:147], v[28:31], v[4:7]\n\t"
    "ds_read_b128 v[124:127], v160 offset:16384\n\t"
    "v_mfma_f32_16x16x32_bf16 v[8:11], v[148:151], v[28:31], v[8:11]\n\t"
    "ds_read_b128 v[128:131], v160 offset:17408\n\t"
    "v_mfma_f32_16x16x32_bf16 v[12:15], v[152:155], v[28:31], v[12:15]\n\t"
    "ds_read_b128 v[132:135], v160 offset:18432\n\t"
    "v_mfma_f32_16x16x32_bf16 v[16:19], v[156:159], v[28:31], v[16:19]\n\t"
    "ds_read_b128 v[136:139], v160 offset:19456\n\t"
    "s_waitcnt vmcnt(20) lgkmcnt(5)\n\t"
    "v_mfma_f32_16x16x32_bf16 v[0:3], v[100:103], v[32:35], v[0:3]\n\t"
    "ds_read_b128 v[140:143], v160 offset:20480\n\t"
    "v_mfma_f32_16x16x32_bf16 v[4:7], v[104:107], v[32:35], v[4:7]\n\t"
    "ds_read_b128 v[144:147], v160 offset:21504\n\t"
    "v_mfma_f32_16x16x32_bf16 v[8:11], v[108:111], v[32:35], v[8:11]\n\t"
    "ds_read_b128 v[148:151], v160 offset:22528\n\t"
    "v_mfma_f32_16x16x32_bf16 v[12:15], v[112:115], v[32:35], v[12:15]\n\t"
    "ds_read_b128 v[152:155], v160 offset:23552\n\t"
    "v_mfma_f32_16x16x32_bf16 v[16:19], v[116:119], v[32:35], v[16:19]\n\t"
    "ds_read_b128 v[156:159], v160 offset:24576\n\t"
    "s_waitcnt vmcnt(19) lgkmcnt(5)\n\t"
    "v_mfma_f32_16x16x32_bf16 v[0:3], v[120:123], v[36:39], v[0:3]\n\t"
    "ds_read_b128 v[100:103], v160 offset:25600\n\t"
    "v_mfma_f32_16x16x32_bf16 v[4:7], v[124:127], v[36:39], v[4:7]\n\t"
    "ds_read_b128 v[104:107], v160 offset:26624\n\t"
    "v_mfma_f32_16x16x32_bf16 v[8:11], v[128:131], v[36:39], v[8:11]\n\t"
    "ds_read_b128 v[108:111], v160 offset:27648\n\t"
    "v_mfma_f32_16x16x32_bf16 v[12:15], v[132:135], v[36:39], v[12:15]\n\t"
    "ds_read_b128 v[112:115], v160 offset:28672\n\t"
    "v_mfma_f32_16x16x32_bf16 v[16:19], v[136:139], v[36:39], v[16:19]\n\t"
    "ds_read_b128 v[116:119], v160 offset:29696\n\t"
    "s_waitcnt vmcnt(18) lgkmcnt(5)\n\t"
    "v_mfma_f32_16x16x32_bf16 v[0:3], v[140:143], v[40:43], v[0:3]\n\t"
    "ds_read_b128 v[120:123], v160 offset:30720\n\t"
    "v_mfma_f32_16x16x32_bf16 v[4:7], v[144:147], v[40:43], v[4:7]\n\t"
    "ds_read_b128 v[124:127], v160 offset:31744\n\t"
    "v_mfma_f32_16x16x32_bf16 v[8:11], v[148:151], v[40:43], v[8:11]\n\t"
    "ds_read_b128 v[128:131], v160 offset:32768\n\t"
    "v_mfma_f32_16x16x32_bf16 v[12:15], v[152:155], v[40:43], v[12:15]\n\t"
    "ds_read_b128 v[132:135], v160 offset:33792\n\t"
    "v_mfma_f32_16x16x32_bf16 v[16:19], v[156:159], v[40:43], v[16:19]\n\t"
    "ds_read_b128 v[136:139], v160 offset:34816\n\t"
    "s_waitcnt vmcnt(17) lgkmcnt(5)\n\t"
    "v_mfma_f32_16x16x32_bf16 v[0:3], v[100:103], v[44:47], v[0:3]\n\t"
    "ds_read_b128 v[140:143], v160 offset:35840\n\t"
    "v_mfma_f32_16x16x32_bf16 v[4:7], v[104:107], v[44:47], v[4:7]\n\t"
    "ds_read_b128 v[144:147], v160 offset:36864\n\t"
    "v_mfma_f32_16x16x32_bf16 v[8:11], v[108:111], v[44:47], v[8:11]\n\t"
    "ds_read_b128 v[148:151], v160 offset:37888\n\t"
    "v_mfma_f32_16x16x32_bf16 v[12:15], v[112:115], v[44:47], v[12:15]\n\t"
    "ds_read_b128 v[152:155], v160 offset:38912\n\t"
    "v_mfma_f32_16x16x32_bf16 v[16:19], v[116:119], v[44:47], v[16:19]\n\t"
    "ds_read_b128 v[156:159], v160 offset:39936\n\t"
    "s_waitcnt vmcnt(16) lgkmcnt(5)\n\t"
    "v_mfma_f32_16x16x32_bf16 v[0:3], v[120:123], v[48:51], v[0:3]\n\t"
    "ds_read_b128 v[100:103], v161 offset:0\n\t"
    "v_mfma_f32_16x16x32_bf16 v[4:7], v[124:127], v[48:51], v[4:7]\n\t"
    "ds_read_b128 v[104:107], v161 offset:1024\n\t"
    "v_mfma_f32_16x16x32_bf16 v[8:11], v[128:131], v[48:51], v[8:11]\n\t"
    "ds_read_b128 v[108:111], v161 offset:2048\n\t"
    "v_mfma_f32_16x16x32_bf16 v[12:15], v[132:135], v[48:51], v[12:15]\n\t"
    "ds_read_b128 v[112:115], v161 offset:3072\n\t"
    "v_mfma_f32_16x16x32_bf16 v[16:19], v[136:139], v[48:51], v[16:19]\n\t"
    "ds_read_b128 v[116:119], v161 offset:4096\n\t"
    "s_waitcnt vmcnt(15) lgkmcnt(5)\n\t"
    "v_mfma_f32_16x16x32_bf16 v[0:3], v[140:143], v[52:55], v[0:3]\n\t"
    "ds_read_b128 v[120:123], v161 offset:5120\n\t"
    "v_mfma_f32_16x16x32_bf16 v[4:7], v[144:147], v[52:55], v[4:7]\n\t"
    "ds_read_b128 v[124:127], v161 offset:6144\n\t"
    "v_mfma_f32_16x16x32_bf16 v[8:11], v[148:151], v[52:55], v[8:11]\n\t"
    "ds_read_b128 v[128:131], v161 offset:7168\n\t"
    "v_mfma_f32_16x16x32_bf16 v[12:15], v[152:155], v[52:55], v[12:15]\n\t"
    "ds_read_b128 v[132:135], v161 offset:8192\n\t"
    "v_mfma_f32_16x16x32_bf16 v[16:19], v[156:159], v[52:55], v[16:19]\n\t"
    "ds_read_b128 v[136:139], v161 offset:9216\n\t"
    "s_waitcnt vmcnt(14) lgkmcnt(5)\n\t"
    "v_mfma_f32_16x16x32_bf16 v[0:3], v[100:103], v[56:59], v[0:3]\n\t"
    "ds_read_b128 v[140:143], v161 offset:10240\n\t"
    "v_mfma_f32_16x16x32_bf16 v[4:7], v[104:107], v[56:59], v[4:7]\n\t"
    "ds_read_b128 v[144:147], v161 offset:11264\n\t"
    "v_mfma_f32_16x16x32_bf16 v[8:11], v[108:111], v[56:59], v[8:11]\n\t"
    "ds_read_b128 v[148:151], v161 offset:12288\n\t"
    "v_mfma_f32_16x16x32_bf16 v[12:15], v[112:115], v[56:59], v[12:15]\n\t"
    "ds_read_b128 v[152:155], v161 offset:13312\n\t"
    "v_mfma_f32_16x16x32_bf16 v[16:19], v[116:119], v[56:59], v[16:19]\n\t"
    "ds_read_b128 v[156:159], v161 offset:14336\n\t"
    "s_waitcnt vmcnt(13) lgkmcnt(5)\n\t"
    "v_mfma_f32_16x16x32_bf16 v[0:3], v[120:123], v[60:63], v[0:3]\n\t"
    "ds_read_b128 v[100:103], v161 offset:15360\n\t"
    "v_mfma_f32_16x16x32_bf16 v[4:7], v[124:127], v[60:63], v[4:7]\n\t"
    "ds_read_b128 v[104:107], v161 offset:16384\n\t"
    "v_mfma_f32_16x16x32_bf16 v[8:11], v[128:131], v[60:63], v[8:11]\n\t"
    "ds_read_b128 v[108:111], v161 offset:17408\n\t"
    "v_mfma_f32_16x16x32_bf16 v[12:15], v[132:135], v[60:63], v[12:15]\n\t"
    "ds_read_b128 v[112:115], v161 offset:18432\n\t"
    "v_mfma_f32_16x16x32_bf16 v[16:19], v[136:139], v[60:63], v[16:19]\n\t"
    "ds_read_b128 v[116:119], v161 offset:19456\n\t"
    "s_waitcnt vmcnt(12) lgkmcnt(5)\n\t"
    "v_mfma_f32_16x16x32_bf16 v[0:3], v[140:143], v[64:67], v[0:3]\n\t"
    "ds_read_b128 v[120:123], v161 offset:20480\n\t"
    "v_mfma_f32_16x16x32_bf16 v[4:7], v[144:147], v[64:67], v[4:7]\n\t"
    "ds_read_b128 v[124:127], v161 offset:21504\n\t"
    "v_mfma_f32_16x16x32_bf16 v[8:11], v[148:151], v[64:67], v[8:11]\n\t"
    "ds_read_b128 v[128:131], v161 offset:22528\n\t"
    "v_mfma_f32_16x16x32_bf16 v[12:15], v[152:155], v[64:67], v[12:15]\n\t"
    "ds_read_b128 v[132:135], v161 offset:23552\n\t"
    "v_mfma_f32_16x16x32_bf16 v[16:19], v[156:159], v[64:67], v[16:19]\n\t"
    "ds_read_b128 v[136:139], v161 offset:24576\n\t"
    "s_waitcnt vmcnt(11) lgkmcnt(5)\n\t"
    "v_mfma_f32_16x16x32_bf16 v[0:3], v[100:103], v[68:71], v[0:3]\n\t"
    "ds_read_b128 v[140:143], v161 offset:25600\n\t"
    "v_mfma_f32_16x16x32_bf16 v[4:7], v[104:107], v[68:71], v[4:7]\n\t"
    "ds_read_b128 v[144:147], v161 offset:26624\n\t"
    "v_mfma_f32_16x16x32_bf16 v[8:11], v[108:111], v[68:71], v[8:11]\n\t"
    "ds_read_b128 v[148:151], v161 offset:27648\n\t"
    "v_mfma_f32_16x16x32_bf16 v[12:15], v[112:115], v[68:71], v[12:15]\n\t"
    "ds_read_b128 v[152:155], v161 offset:28672\n\t"
    "v_mfma_f32_16x16x32_bf16 v[16:19], v[116:119], v[68:71], v[16:19]\n\t"
    "ds_read_b128 v[156:159], v161 offset:29696\n\t"
    "s_waitcnt vmcnt(10) lgkmcnt(5)\n\t"
    "v_mfma_f32_16x16x32_bf16 v[0:3], v[120:123], v[72:75], v[0:3]\n\t"
    "ds_read_b128 v[100:103], v161 offset:30720\n\t"
    "v_mfma_f32_16x16x32_bf16 v[4:7], v[124:127], v[72:75], v[4:7]\n\t"
    "ds_read_b128 v[104:107], v161 offset:31744\n\t"
    "v_mfma_f32_16x16x32_bf16 v[8:11], v[128:131], v[72:75], v[8:11]\n\t"
    "ds_read_b128 v[108:111], v161 offset:32768\n\t"
    "v_mfma_f32_16x16x32_bf16 v[12:15], v[132:135], v[72:75], v[12:15]\n\t"
    "ds_read_b128 v[112:115], v161 offset:33792\n\t"
    "v_mfma_f32_16x16x32_bf16 v[16:19], v[136:139], v[72:75], v[16:19]\n\t"
    "ds_read_b128 v[116:119], v161 offset:34816\n\t"
    "s_waitcnt vmcnt(9) lgkmcnt(5)\n\t"
    "v_mfma_f32_16x16x32_bf16 v[0:3], v[140:143], v[76:79], v[0:3]\n\t"
    "ds_read_b128 v[120:123], v161 offset:35840\n\t"
    "v_mfma_f32_16x16x32_bf16 v[4:7], v[144:147], v[76:79], v[4:7]\n\t"
    "ds_read_b128 v[124:127], v161 offset:36864\n\t"
    "v_mfma_f32_16x16x32_bf16 v[8:11], v[148:151], v[76:79], v[8:11]\n\t"
    "ds_read_b128 v[128:131], v161 offset:37888\n\t"
    "v_mfma_f32_16x16x32_bf16 v[12:15], v[152:155], v[76:79], v[12:15]\n\t"
    "ds_read_b128 v[132:135], v161 offset:38912\n\t"
    "v_mfma_f32_16x16x32_bf16 v[16:19], v[156:159], v[76:79], v[16:19]\n\t"
    "ds_read_b128 v[136:139], v161 offset:39936\n\t"
    "s_waitcnt vmcnt(8) lgkmcnt(5)\n\t"
    "v_mfma_f32_16x16x32_bf16 v[0:3], v[100:103], v[80:83], v[0:3]\n\t"
    "ds_read_b128 v[140:143], v161 offset:40960\n\t"
    "v_mfma_f32_16x16x32_bf16 v[4:7], v[104:107], v[80:83], v[4:7]\n\t"
    "ds_read_b128 v[144:147], v161 offset:41984\n\t"
    "v_mfma_f32_16x16x32_bf16 v[8:11], v[108:111], v[80:83], v[8:11]\n\t"
    "ds_read_b128 v[148:151], v161 offset:43008\n\t"
    "v_mfma_f32_16x16x32_bf16 v[12:15], v[112:115], v[80:83], v[12:15]\n\t"
    "ds_read_b128 v[152:155], v161 offset:44032\n\t"
    "v_mfma_f32_16x16x32_bf16 v[16:19], v[116:119], v[80:83], v[16:19]\n\t"
    "ds_read_b128 v[156:159], v161 offset:45056\n\t"
    "s_waitcnt vmcnt(6) lgkmcnt(5)\n\t"
    "v_mfma_f32_16x16x32_bf16 v[0:3], v[120:123], v[84:87], v[0:3]\n\t"
    "ds_read_b128 v[100:103], v161 offset:46080\n\t"
    "v_mfma_f32_16x16x32_bf16 v[4:7], v[124:127], v[84:87], v[4:7]\n\t"
    "ds_read_b128 v[104:107], v161 offset:47104\n\t"
    "v_mfma_f32_16x16x32_bf16 v[8:11], v[128:131], v[84:87], v[8:11]\n\t"
    "ds_read_b128 v[108:111], v161 offset:48128\n\t"
    "v_mfma_f32_16x16x32_bf16 v[12:15], v[132:135], v[84:87], v[12:15]\n\t"
    "ds_read_b128 v[112:115], v161 offset:49152\n\t"
    "v_mfma_f32_16x16x32_bf16 v[16:19], v[136:139], v[84:87], v[16:19]\n\t"
    "ds_read_b128 v[116:119], v161 offset:50176\n\t"
    "s_waitcnt vmcnt(4) lgkmcnt(5)\n\t"
    "v_mfma_f32_16x16x32_bf16 v[0:3], v[140:143], v[88:91], v[0:3]\n\t"
    "ds_read_b128 v[120:123], v161 offset:51200\n\t"
    "v_mfma_f32_16x16x32_bf16 v[4:7], v[144:147], v[88:91], v[4:7]\n\t"
    "ds_read_b128 v[124:127], v161 offset:52224\n\t"
    "v_mfma_f32_16x16x32_bf16 v[8:11], v[148:151], v[88:91], v[8:11]\n\t"
    "ds_read_b128 v[128:131], v161 offset:53248\n\t"
    "v_mfma_f32_16x16x32_bf16 v[12:15], v[152:155], v[88:91], v[12:15]\n\t"
    "ds_read_b128 v[132:135], v161 offset:54272\n\t"
    "v_mfma_f32_16x16x32_bf16 v[16:19], v[156:159], v[88:91], v[16:19]\n\t"
    "ds_read_b128 v[136:139], v161 offset:55296\n\t"
    "s_waitcnt vmcnt(2) lgkmcnt(5)\n\t"
    "v_mfma_f32_16x16x32_bf16 v[0:3], v[100:103], v[92:95], v[0:3]\n\t"
    "v_mfma_f32_16x16x32_bf16 v[4:7], v[104:107], v[92:95], v[4:7]\n\t"
    "v_mfma_f32_16x16x32_bf16 v[8:11], v[108:111], v[92:95], v[8:11]\n\t"
    "v_mfma_f32_16x16x32_bf16 v[12:15], v[112:115], v[92:95], v[12:15]\n\t"
    "v_mfma_f32_16x16x32_bf16 v[16:19], v[116:119], v[92:95], v[16:19]\n\t"
    "s_waitcnt vmcnt(0) lgkmcnt(0)\n\t"
    "v_mfma_f32_16x16x32_bf16 v[0:3], v[120:123], v[96:99], v[0:3]\n\t"
    "v_mfma_f32_16x16x32_bf16 v[4:7], v[124:127], v[96:99], v[4:7]\n\t"
    "v_mfma_f32_16x16x32_bf16 v[8:11], v[128:131], v[96:99], v[8:11]\n\t"
    "v_mfma_f32_16x16x32_bf16 v[12:15], v[132:135], v[96:99], v[12:15]\n\t"
    "v_mfma_f32_16x16x32_bf16 v[16:19], v[136:139], v[96:99], v[16:19]\n\t"
    "s_nop 7\n\t"
    "v_cvt_pk_bf16_f32 v24, v0, v1\n\t"
    "v_cvt_pk_bf16_f32 v25, v2, v3\n\t"
    "v_cvt_pk_bf16_f32 v26, v4, v5\n\t"
    "v_cvt_pk_bf16_f32 v27, v6, v7\n\t"
    "v_cvt_pk_bf16_f32 v28, v8, v9\n\t"
    "v_cvt_pk_bf16_f32 v29, v10, v11\n\t"
    "v_cvt_pk_bf16_f32 v30, v12, v13\n\t"
    "v_cvt_pk_bf16_f32 v31, v14, v15\n\t"
    "v_cvt_pk_bf16_f32 v32, v16, v17\n\t"
    "v_cvt_pk_bf16_f32 v33, v18, v19\n\t"
    "v_permlane16_swap_b32 v24, v26\n\t"
    "v_permlane16_swap_b32 v25, v27\n\t"
    "v_permlane16_swap_b32 v28, v30\n\t"
    "v_permlane16_swap_b32 v29, v31\n\t"
    "global_store_dwordx4 v164, v[24:27], %[out]\n\t"
    "global_store_dwordx4 v164, v[28:31], %[out] offset:64\n\t"
    "global_store_dwordx2 v165, v[32:33], %[out] offset:128\n\t"
    : "+{v[88:91]}"(rt[0]), "+{v[92:95]}"(rt[1]), "+{v[96:99]}"(rt[2]), "+{v[100:103]}"(rt[3]), "+{v[104:107]}"(rt[4]), "+{v80}"(p1), "+{v81}"(p2), "+{v82}"(p3), "+{v83}"(p4),
      "+{v152}"(h0), "+{v153}"(h1), "+{v154}"(h2), "+{v155}"(h3), "+{v156}"(l16), "+{v157}"(ot), "+{v158}"(ot2), "+{v159}"(xo),
      [cnt] "=&s"(cnt), [sp] "=&s"(sp), [t0] "=&s"(t0)
    : "{v160}"(la), "{v161}"(lb), "{v162}"(pf2), "{v163}"(LDS_CNT), [ws] "s"(ws), [rs] "s"(rs), [tag] "s"(tag), [tl] "s"((unsigned)tag), [th] "s"((unsigned)(tag >> 32)),
      [kv] "s"(kv), [qb] "s"(qb), [mk] "s"(LDS_KV + kq * 5120), [mq] "s"(kq * 2560), [mw] "s"(kq * 5120), [pub] "s"(pub), [epo] "s"(epo), [rto] "s"(rto), [rfo] "s"(rfo),
      [wt] "s"(wt), [hb] "s"(hb), [wb] "s"(wb), [xb] "s"(xb), [w0] "s"(w0), [ob] "s"(ob), [tb] "s"(tb), [out] "s"(out),
      [r0] "s"(LDS_KV + kq * 15360), [r1] "s"(LDS_KV + ((kq + 1) & 3) * 15360), [r2] "s"(LDS_KV + ((kq + 2) & 3) * 15360 + 5120), [r3] "s"(LDS_KV + ((kq + 3) & 3) * 15360 + 10240)
    : "v0", "v1", "v2", "v3", "v4", "v5", "v6", "v7", "v8", "v9", "v10", "v11", "v12", "v13", "v14", "v15", "v16", "v17", "v18", "v19", "v20", "v21", "v22", "v23", "v24", "v25", "v26", "v27", "v28", "v29", "v30", "v31", "v32", "v33", "v34", "v35", "v36", "v37", "v38", "v39", "v40", "v41", "v42", "v43", "v44", "v45", "v46", "v47", "v48", "v49", "v50", "v51", "v52", "v53", "v54", "v55", "v56", "v57", "v58", "v59", "v60", "v61", "v62", "v63", "v64", "v65", "v66", "v67", "v68", "v69", "v70", "v71", "v72", "v73", "v74", "v75", "v76", "v77", "v78", "v79", "v84", "v85", "v86", "v87", "v108", "v109", "v110", "v111", "v112", "v113", "v114", "v115", "v116", "v117", "v118", "v119", "v120", "v121", "v122", "v123", "v124", "v125", "v126", "v127", "v128", "v129", "v130", "v131", "v132", "v133", "v134", "v135", "v136", "v137", "v138", "v139", "v140", "v141", "v142", "v143", "v144", "v145", "v146", "v147", "v148", "v149", "v150", "v151", "v164", "v165", "v166", "v167", "s44", "s45", "s46", "s47", "vcc", "scc", "memory");
}

typedef __attribute__((address_space(1))) u64 gu64;
constexpr u64 EPOCH_STEP = 0x9E3779B97F4A7C15ull;
__device__ __forceinline__ void flag_set(char* ws, int idx, u64 tag, int lane) {
  if (lane == 0) __hip_atomic_store((gu64*)(ws + WS_FLAG + idx * 128), tag, __ATOMIC_RELAXED, __HIP_MEMORY_SCOPE_AGENT);
}
__device__ __forceinline__ void flag_wait(char* ws, int producer, u64 tag) {
  const unsigned off = WS_FLAG + producer * 128;
  u64 v, ex; unsigned sp;
  asm volatile("s_mov_b64 %[ex], exec\n\tv_cmpx_le_i32 vcc, 0, %[pr]\n\ts_mov_b32 %[sp], 0x40000\n"
               "Lw_%=:\n\tglobal_load_dwordx2 %[v], %[off], %[ws] sc1\n\ts_waitcnt vmcnt(0)\n\tv_cmp_ne_u64 vcc, %[tag], %[v]\n\ts_cbranch_vccz Ld_%=\n\t"
               "s_sleep 1\n\ts_sub_u32 %[sp], %[sp], 1\n\ts_cbranch_scc0 Lw_%=\n"
               "Ld_%=:\n\ts_mov_b64 exec, %[ex]"
               : [v] "=&v"(v), [ex] "=&s"(ex), [sp] "=&s"(sp) : [pr] "v"(producer), [off] "v"(off), [ws] "s"(ws), [tag] "s"(tag) : "vcc", "scc", "memory");
}
__device__ __forceinline__ u64 read_tag(char* lds) {
  const u32x2 t = *(LAS u32x2*)(lds + LDS_TAG);
  return (u64)(unsigned)__builtin_amdgcn_readfirstlane(t[1]) << 32 | (unsigned)__builtin_amdgcn_readfirstlane(t[0]);
}
__device__ __forceinline__ const unsigned short* pf_addr(const unsigned short* pa, const unsigned short* pb, int ld, bool qkv, int sl, int sub, int lane) {
  const int id = min(lane, 59), r6 = (id * 10923) >> 16, k6 = id - r6 * 6;
  const int col = qkv ? min(k6, 4) * 64 : min((k6 < 3 ? k6 : k6 - 3) * 64, 159);
  return (k6 < 3 ? pa : pb) + (unsigned)((80 * sl + sub + r6 * 8) * ld + col);
}
__device__ __forceinline__ u64 pf_mid(const void* p) { return ((u64)p & ~127ull) + 60; }
#define PF_LOAD(p) asm volatile("global_load_dwordx2 %0, %1, off" : "+{v[166:167]}"(pf) : "v"(pf_mid(p)) : "memory")
#define W_DELAY 2
__device__ __forceinline__ unsigned lds_arrive(int c) {
  unsigned v, n;
  asm volatile("s_waitcnt lgkmcnt(0)\n\ts_mov_b64 exec, 1\n\tds_add_rtn_u32 %0, %2, %3\n\ts_mov_b64 exec, -1\n\ts_waitcnt lgkmcnt(0)\n\tv_readfirstlane_b32 %1, %0"
               : "=&v"(v), "=s"(n) : "v"(LDS_CNT + 4 * c), "v"(1) : "memory");
  return n;
}
__device__ __forceinline__ void lds_wait(int c, unsigned target) {
  unsigned v, n, sp;
  asm volatile("s_mov_b32 %2, 0x100000\n"
               "Lw_%=:\n\tds_read_b32 %0, %3\n\ts_waitcnt lgkmcnt(0)\n\tv_readfirstlane_b32 %1, %0\n\ts_cmp_ge_u32 %1, %4\n\ts_cbranch_scc1 Ld_%=\n\t"
               "s_sleep 1\n\ts_sub_u32 %2, %2, 1\n\ts_cbranch_scc0 Lw_%=\n"
               "Ld_%=:"
               : "=&v"(v), "=&s"(n), "=&s"(sp) : "v"(LDS_CNT + 4 * c), "s"(target) : "scc", "memory");
}
constexpr int CODE_LO = -384, CODE_HI = 38272;
#define MPF 160
#define DRAIN() asm volatile("s_waitcnt vmcnt(0)" ::: "memory")
__device__ __forceinline__ int fresh_lane() { int l; asm volatile("v_mbcnt_lo_u32_b32 %0, -1, 0\n\tv_mbcnt_hi_u32_b32 %0, -1, %0" : "=v"(l)); return l; }
__device__ __forceinline__ int cb_of_slot(int s) { return (int)((0xFED5CBA398147620ull >> (4 * s)) & 15); }
__device__ __forceinline__ int g1_block(int rp, int cb) {
  const int s = (int)((0xFEDBA97632C48150ull >> (4 * cb)) & 15);
  return (((rp & 7) | ((s & 3) << 3)) << 3) | (rp >> 3) | ((s >> 2) << 1);
}
__device__ __forceinline__ float sum8(float v) {
  v += __builtin_bit_cast(float, __builtin_amdgcn_update_dpp(0, __builtin_bit_cast(int, v), 0xB1, 0xF, 0xF, true));
  v += __builtin_bit_cast(float, __builtin_amdgcn_update_dpp(0, __builtin_bit_cast(int, v), 0x4E, 0xF, 0xF, true));
  v += __builtin_bit_cast(float, __builtin_amdgcn_update_dpp(0, __builtin_bit_cast(int, v), 0x141, 0xF, 0xF, true));
  return v;
}
__device__ __forceinline__ unsigned wtile_addr(int G, int g, int q) {
  const int s = (q >> 1) | ((g & 1) << 1);
  return 40960 + ((G >> 4) * 4 + g) * 1024 + q * 128 + ((((G >> 2) & 3) ^ s) << 5) + (G & 3) * 8;
}
__device__ __forceinline__ u32x2 ld8(const char* p) {
  return __builtin_bit_cast(u32x2, __hip_atomic_load((gu64*)p, __ATOMIC_RELAXED, __HIP_MEMORY_SCOPE_AGENT));
}
__device__ __forceinline__ u32x4 ld16(const char* p) { const u32x2 a = ld8(p), c = ld8(p + 8); return u32x4{a[0], a[1], c[0], c[1]}; }
__device__ __forceinline__ f32x16 att_safe(const char* qp, int sub, const char* kv, int lane) {
  const int rw = lane & 31, h = lane >> 5;
  const bf16x8 qm = __builtin_bit_cast(bf16x8, ld16(qp + sub * 1024 + h * 512 + rw * 16));
  u32x2 t2 = ld8(qp + 2048 + rw * 16 + sub * 8);
  if (h) t2 = u32x2{0, 0};
  const bf16x8 qt = __builtin_bit_cast(bf16x8, u32x4{t2[0], t2[1], 0, 0});
  const unsigned one = (rw == 20 || rw == 24) ? 0x3f803f80u : 0u;
  f32x16 o = {};
  float m = -__builtin_inff();
#pragma unroll 1
  for (int t = 0; t < 64; ++t) {
    const char* tb = kv + (size_t)(t >> 1) * 5120;
    const bf16x8 km = __builtin_bit_cast(bf16x8, ld16(tb + (t & 1) * 1024 + h * 512 + rw * 16));
    const u32x2 k2 = ld8(tb + 2048 + rw * 16 + (t & 1) * 8);
    u32x4 v0 = {one, one, one, one}, v1 = v0;
    const char* vb = tb + 2560 + (t & 1) * 1280 + h * 320 + rw * 16;
    if (rw < 20) { v0 = ld16(vb); v1 = ld16(vb + 640); }
    f32x16 s = __builtin_amdgcn_mfma_f32_32x32x16_bf16(km, qm, f32x16{}, 0, 0, 0);
    s = __builtin_amdgcn_mfma_f32_32x32x16_bf16(__builtin_bit_cast(bf16x8, u32x4{k2[0], k2[1], 0, 0}), qt, s, 0, 0, 0);
    float mx = m;
#pragma unroll
    for (int e = 0; e < 16; ++e) mx = fmaxf(mx, s[e]);
    mx = fmaxf(mx, __shfl_xor(mx, 32));
    const float al = __builtin_amdgcn_exp2f(m - mx);
    m = mx;
#pragma unroll
    for (int e = 0; e < 16; ++e) { o[e] *= al; s[e] = __builtin_amdgcn_exp2f(s[e] - mx); }
    const f32x8 p0 = __builtin_shufflevector(s, s, 0, 1, 2, 3, 4, 5, 6, 7), p1 = __builtin_shufflevector(s, s, 8, 9, 10, 11, 12, 13, 14, 15);
    o = __builtin_amdgcn_smfmac_f32_32x32x32_bf16(__builtin_bit_cast(bf16x8, v0), __builtin_bit_cast(bf16x16, p0), o, 0xdddd, 0, 0);
    o = __builtin_amdgcn_smfmac_f32_32x32x32_bf16(__builtin_bit_cast(bf16x8, v1), __builtin_bit_cast(bf16x16, p1), o, 0xdddd, 0, 0);
  }
  return o;
}

__global__ void __launch_bounds__(768) fused_kernel(const unsigned short* __restrict__ x, const unsigned short* __restrict__ wqkv,
                                                    const unsigned short* __restrict__ wo, const unsigned short* __restrict__ w1,
                                                    const unsigned short* __restrict__ w3, const unsigned short* __restrict__ w2,
                                                    char* __restrict__ ws, unsigned short* __restrict__ out) {
  extern __shared__ __attribute__((aligned(16))) char lds[];
  const int w = __builtin_amdgcn_readfirstlane(threadIdx.x >> 6);
  const int b = blockIdx.x, xcd = b & 7, bi = b >> 3;
  STAMP(0);
  u32x4 v[2];
  u64 ep;
  if (w < 5) {
    const int lane = threadIdx.x & 63;
    if (w == 0) asm volatile("global_load_dwordx2 %0, %1, %2 sc1" : "=v"(ep) : "v"((unsigned)WS_EPOCH), "s"(ws) : "memory");
    const unsigned short* xr = x + (b * 8 * D + w * 128) + (unsigned)((lane >> 3) * D + (lane & 7) * 8);
    v[0] = *(const u32x4*)xr; v[1] = *(const u32x4*)(xr + 64);
  }
  asm volatile("" ::: "memory");
  const int rp = (xcd & 1) * 8 + (bi & 7), cb = cb_of_slot((xcd >> 1) * 4 + (bi >> 3));
  int cbo = cb;
  asm volatile("" : "+s"(cbo));
  const int jb = cbo - 6;
  const int f0 = jb < 9 ? 160 * jb : F - 160;
  const rsrc_t rs = ws_rsrc(ws);
  const unsigned short* pfa = cb < 6 ? wqkv + cb * 320 : w1 + f0;
  const unsigned short* pfb = cb < 6 ? pfa : w3 + f0;
  const int pfl = cb < 6 ? 1920 : F;
  u64 pf = 0;
  u64 tag;
  if (w >= 8) {
    const int lw = w - 8;
    const int ru = rp * 10 + jb, step = min(ru, 135) >> 1, half = ru & 1;
    const bool retile = cb >= 6 && ru < 136;
    u32x4 rt[5] = {};
    {
      const int lane = fresh_lane(), rr = lane >> 3, u = lane & 7;
      const int grp = (u >> 1) ^ (((rr >> 1) & 1) | ((lw & 1) << 1)), krow = 8 * lw + rr;
      const int ce = grp * 16 + (u & 1) * 8, ce3 = ce ^ 8;
      const u64 xb = (u64)(ws + WS_XN) + (size_t)rp * 8 * 20480;
      const unsigned vx0 = lw * 20480 + lane * 16;
      const int fa = f0 + (f0 & 32), fh = f0 & 32 ? f0 : f0 + 128;
      const u64 wb = cb < 6 ? (u64)(wqkv + cb * 320) : (u64)(w1 + fa), w3b = (u64)(w3 + fa);
      const u64 vs = grp < 2 ? (u64)(w1 + krow * F + fh + ce) : (u64)(w3 + krow * F + fh - 32 + ce3);
      const unsigned vw = (krow * (cb < 6 ? 1920 : F) + ce) * 2, vw3 = (krow * F + ce3) * 2;
      const int q5 = (step * 205) >> 10, c5 = step - q5 * 5;
      const bool rest = step < 45 && c5 == 4, tl = step >= 64, wom = step >= 48 && !tl;
      int r0 = 160 * q5 + (rest ? 32 : 40 * c5);
      r0 = step >= 45 ? 32 * step : r0; r0 = wom ? 40 * (step - 48) : r0; r0 = tl ? 160 * (step - 64) + 16 : r0;
      const int c1 = rest ? 4 : tl ? 20 : wom ? 8 : 16, c2 = rest || tl ? 40 : 4, c3 = rest || tl ? 80 : wom ? 20 : 8;
      const u64 rsrc0 = (u64)((step < 48 ? w2 : wo) + r0 * D + half * 320 + lw * 80);
      unsigned ro[5];
#pragma unroll
      for (int i = 0; i < 5; ++i) {
        const int p = lane + i * 64, kk = (p * 6554) >> 16, cc = p - kk * 10;
        ro[i] = ((c1 * ((kk >> 2) & 1) + c2 * ((kk >> 3) & 1) + c3 * (kk >> 4) + (kk & 3)) * D + cc * 8) * 2;
      }
      if (lw < 2) PF_LOAD(pf_addr(pfa, pfb, pfl, cb < 6, lw, bi & 7, lane));
      asm volatile("" : "+{v108}"(ro[0]), "+{v109}"(ro[1]), "+{v110}"(ro[2]), "+{v111}"(ro[3]), "+{v112}"(ro[4]));
      BARRIER();
      tag = read_tag(lds);
      BARRIER();
      __builtin_amdgcn_s_sleep(W_DELAY);
      __builtin_amdgcn_s_setprio(3);
      if (cb < 6) k1_pre_qkv(xb, wb, w3b, vx0, vw, vw3, vs, lw, 32 * 1920 * 2); else k1_pre_ffn(xb, wb, w3b, vx0, vw, vw3, vs, lw, 32 * F * 2);
      asm volatile("s_waitcnt vmcnt(25)" : "+{v[166:167]}"(pf) :: "memory");
      BARRIER();
      STAMP(1);
      if (cb < 6) k1_load_qkv(xb, wb, w3b, vx0, vw, vw3, vs, lw, 32 * 1920 * 2); else k1_load_ffn(xb, wb, w3b, vx0, vw, vw3, vs, lw, 32 * F * 2, rt, rsrc0, ro);
    }
    const int kq = lw, rb = (b & 7) * 4 + ((b >> 3) & 3), cn = b >> 5;
    {
      const int lane = fresh_lane();
      {
        STAMP(2);
        lds_wait(0, 8);
        if (retile) {
          LAS char* stg = (LAS char*)lds + LDS_HI + lw * 5120;
          LAS char* trp = stg + (8 * (lane >> 4) + ((lane >> 2) & 3)) * 160 + (lane & 3) * 8;
#pragma unroll
          for (int i = 0; i < 5; ++i) *(LAS u32x4*)(stg + (lane + i * 64) * 16) = rt[i];
#pragma unroll
          for (int i = 0; i < 5; ++i) {
            const u32x2 lo = __builtin_bit_cast(u32x2, __builtin_amdgcn_ds_read_tr16_b64_v4i16((LAS s16x4*)(trp + i * 32)));
            const u32x2 hi = __builtin_bit_cast(u32x2, __builtin_amdgcn_ds_read_tr16_b64_v4i16((LAS s16x4*)(trp + i * 32 + 640)));
            rt[i] = u32x4{lo[0], lo[1], hi[0], hi[1]};
          }
        }
        lds_arrive(2);
        STAMP(5);
      }
      const int head = (b & 7) + 8 * ((b >> 3) & 3), hh = head >> 4;
      auto foff = [&](int idx) { return (unsigned)(WS_FLAG + idx * 128); };
      auto qkv = [&](int pair, int part) { return F_QKV + g1_block(pair >> 1, 2 * part + hh) * 8 + (pair & 1) * 4 + ((head >> 2) & 3); };
      const int unit = lane < 22 ? kq * 12 + max(lane - 10, 0) : 48 + (lane >= 27 ? 0 : min(kq + 1 + (lane - 22) * 4, 19));
      const size_t row0 = (size_t)(rb * 64 + kq * 16) * D + cn * 80;
      ffn_tail(ws, rs, tag, kq,
               foff(lane < 2 ? qkv(lw, 1 + lane) : qkv(cn * 4 + lw, 0)),
               foff(qkv(4 + (lane & 2) * 2 + lw, 1 + (lane & 1))),
               foff(qkv((12 + lw * 5 + (lane >> 1)) & 31, 1 + (lane & 1))),
               foff(lane < 10 ? F_H + g1_block(rb >> 1, 6 + lane) : F_RT + (unit * 2 + (cn >> 2)) * 4 + (cn & 3)),
               foff(F_O + ((rb >> 2) * 32 + (lane & 31)) * 8 + (kq >> 1) * 4 + (rb & 3)),
               rt, retile ? (unsigned)(WS_W2T + (step * 40 + half * 20 + lw * 5) * 1024) : 0u, foff(F_RT + ru * 4 + lw),
               (kq & 3) * 49152 + lane * 16, ((kq + 1) & 3) * 49152 + lane * 16, ((kq + 2) & 3) * 49152 + lane * 16, ((kq + 3) & 3) * 49152 + lane * 16,
               lane * 16, (lane >> 4) * 32768 + (lane & 15) * 8, (lane & 15) * 1280 + (lane >> 4) * 8,
               (u64)(ws + WS_KV) + (size_t)(head * 32 + lw) * 5120, (u64)(ws + WS_Q) + (size_t)(head * 32 + cn * 4 + lw) * 2560,
               lw == 0 && cb >= 6 ? foff(F_H + b) : 0u,
               lw == 0 && b == 0 ? (unsigned)WS_EPOCH : 0u,
               (u64)(ws + WS_WOT) + (size_t)((kq + 1) * 40 + cn * 5) * 1024,
               (u64)(ws + WS_H) + (size_t)(rb * 4 * 48 + kq * 12) * 1024, (u64)(ws + WS_W2T) + (size_t)(kq * 12 * 40 + cn * 5 + 2) * 1024,
               (u64)(x + row0), (u64)(ws + WS_WOT) + (size_t)(cn * 5 + 2) * 1024, (u64)(ws + WS_O) + (size_t)(rb * 4 + kq) * 16384 + 2048,
               (u64)(ws + WS_OT) + (size_t)(rb * 64 + kq * 16) * 8, (u64)(out + row0) DGARG);
      STAMP(12);
      FLUSH(0x1ffff);
    }
    return;
  }
  u64 pc0;
  asm volatile("s_getpc_b64 %0" : "=s"(pc0));
  pc0 &= ~127ull;
  {
    const int tid = threadIdx.x, lane = tid & 63;
    const int tok = lane >> 3, c = lane & 7;
    LAS f32x2* red = (LAS f32x2*)(lds + LDS_LN);
    if (w < 5) {
      float sum = 0.f;
#pragma unroll
      for (int s = 0; s < 2; ++s)
#pragma unroll
        for (int e = 0; e < 4; ++e) sum += bflo(v[s][e]) + bfhi(v[s][e]);
      const float mw = sum8(sum) * (1.0f / 128);
      float sq = 0.f;
#pragma unroll
      for (int s = 0; s < 2; ++s)
#pragma unroll
        for (int e = 0; e < 4; ++e) { const float a = bflo(v[s][e]) - mw, c2_ = bfhi(v[s][e]) - mw; sq += a * a + c2_ * c2_; }
      sq = sum8(sq);
      if (c == 0) red[w * 8 + tok] = f32x2{mw, sq};
      if (w == 0) {
        asm volatile("s_waitcnt vmcnt(0)" : "+v"(ep) :: "memory");
        if (lane == 0) *(LAS u64*)(lds + LDS_TAG) = ep + EPOCH_STEP;
      }
    } else {
      if (bi < 8)
      for (int i = 0; i < 2; ++i) PF_LOAD((const char*)pc0 + min(((w - 5) + i * 3) * 8192 + lane * 128, CODE_HI - 128));
    }
    STAMP(1);
    BARRIER();
    tag = read_tag(lds);
    if (w >= 5) {
      const int i = b * 3 + w - 5, nq = (640 - MPF) * 30 / 64, nf = (640 - MPF) * 24 / 64;
      const char* p = i < nq ? (const char*)(wqkv + MPF * 1920) + i * 8192 : i < nq + nf ? (const char*)(w1 + MPF * F) + (i - nq) * 8192 : (const char*)(w3 + MPF * F) + (i - nq - nf) * 8192;
      if (i < nq + 2 * nf) PF_LOAD(p + lane * 128);
    }
    if (w < 5) {
      f32x2 pt[5];
#pragma unroll
      for (int i = 0; i < 5; ++i) pt[i] = red[i * 8 + tok];
      const float mean = (pt[0][0] + pt[1][0] + pt[2][0] + pt[3][0] + pt[4][0]) * 0.2f;
      float m2 = 0.f;
#pragma unroll
      for (int i = 0; i < 5; ++i) { const float d = pt[i][0] - mean; m2 += pt[i][1] + 128.0f * d * d; }
      const float mb = rbf(mean), inv = __builtin_amdgcn_rcpf(rbf(__builtin_amdgcn_sqrtf(rbf(rbf(m2 * (1.0f / D)) + 1e-5f))));
      const unsigned dst = WS_XN + (((b >> 1) * 20 + w * 4 + (c >> 2)) * 64 + (b & 1) * 8 + tok + 16 * (c & 3)) * 16;
#pragma unroll
      for (int s = 0; s < 2; ++s) {
        u32x4 o;
#pragma unroll
        for (int e = 0; e < 4; ++e) { const unsigned t = pk2(bflo(v[s][e]) - mb, bfhi(v[s][e]) - mb); o[e] = pk2(bflo(t) * inv, bfhi(t) * inv); }
        st16(rs, dst + s * 2048, o);
      }
    }
    STAMP(2);
    if (w < 5) DRAIN();
    STAMP(3);
    BARRIER();
    if (w == 0) flag_set(ws, F_XN + b, tag, lane);
    STAMP(4);
  }
  {
    DGDECL;
    const int rg = w >> 2, cg = w & 3;
    {
      const int lane = fresh_lane(), g = lane >> 4, q = (lane >> 2) & 3, p = lane & 3;
      const int jw = jb < 9 ? 5 * cg : (cg < 3 ? 8 + 4 * cg : 0);
      unsigned vw[5];
#pragma unroll
      for (int n = 0; n < 5; ++n) {
        int G;
        if (cb < 4) G = n < 4 ? 20 * cg + 5 * n + p : 20 * cg + 5 * p + 4;
        else if (cb < 6) G = 20 * cg + 4 * n + p;
        else { const int j = min(jw + n, 19), U = f0 & 32 ? (j < 4 ? j + 16 : j - 4) : j; G = p < 2 ? 2 * U + p : (U < 16 ? 48 : 8) + 2 * (U ^ 1) + (p & 1); }
        vw[n] = wtile_addr(G, g, q);
      }
      const unsigned hp = (16 * (cb & 1) + 4 * cg) * 32 + rp * 2 + rg;
      const unsigned hb = WS_H + (rp * 2 + rg) * 4 * 48 * 1024;
      const unsigned ob = cb < 2 ? WS_Q + hp * 2560 : cb < 6 ? WS_KV + hp * 5120 + (cb < 4 ? 0 : 2560) : jb < 9 ? hb + (5 * jb + cg) * 1024 : cg < 3 ? hb + (45 + cg) * 1024 : 0;
      const unsigned o2 = jb < 9 ? hb + (5 * jb + 4) * 1024 + cg * 256 : 0;
      u32x8 ha = u32x8{(unsigned)(rg * 4096 + lane * 16), vw[0], vw[1], vw[2], vw[3], vw[4], 0, 0};
      asm volatile("" : "+{v[152:159]}"(ha));
      if (w == 0) {
        __builtin_amdgcn_s_sleep(8);
        flag_wait(ws, lane < 16 ? F_XN + rp * 16 + lane : -1, tag);
      }
      BARRIER();
      STAMP(5);
      asm volatile("s_waitcnt vmcnt(0)" : "+{v[166:167]}"(pf) :: "memory");
      k1_main(ha, w, cb >> 1, rs, ob, o2 DGARG);
      STAMP(10); CSTAMP(6, dg[0]); CSTAMP(7, dg[1]); CSTAMP(8, dg[2]); CSTAMP(9, dg[3]);
    }
    const int lane = fresh_lane();
    STAMP(11);
    __builtin_amdgcn_s_setprio(0);
    if (cb < 6) {
      DRAIN();
      flag_set(ws, F_QKV + b * 8 + w, tag, lane);
    }
  }
  {
    const int head = (b & 7) + 8 * ((b >> 3) & 3), qc = b >> 5, kh = w >> 2, qg = w & 3;
    const char* qp = ws + WS_Q + (size_t)(head * 32 + qc * 4 + qg) * 2560;
    const char* kv = ws + WS_KV + (size_t)head * 32 * 5120;
    const int tk0 = qc * 256 + qg * 64 + kh * 32;
    const unsigned so = WS_O + ((tk0 >> 4) * 16 + (head >> 1)) * 1024 + (head & 1) * 512, st = WS_OT + (head * 2048 + tk0) * 8;
    u64 ok;
    {
      DGDECL;
      const int lane = fresh_lane(), rw = lane & 31, h = lane >> 5;
      STAMP(12);
      ok = att_main(qg * 2560 + h * 512 + rw * 16, qg * 2560 + 2048 + rw * 16 + h * 8,
                    LDS_KV + kh * 10240 + h * 512 + rw * 16, LDS_KV + kh * 10240 + 2048 + rw * 16, LDS_KV + kh * 10240 + 2560 + h * 320 + rw * 16,
                    w * 1024 + lane * 16, (u64)kv + 3 * 20480, w, (rw == 20 || rw == 24) ? 0x3f803f80u : 0u,
                    rs, (rw >> 4) * 16384 + (rw & 15) * 16 + h * 256, rw * 8, so, st, WS_FLAG + (F_O + b * 8 + w) * 128, tag DGARG);
    }
    if (__builtin_expect(ok != ~0ull, 0)) {
      const int lane = fresh_lane(), rw = lane & 31, h = lane >> 5;
      const f32x16 o = att_safe(qp, kh, kv, lane);
      const float rl = 1.0f / (h ? o[8] : o[12]);
      st16(rs, so + (rw >> 4) * 16384 + (rw & 15) * 16 + h * 256,
           u32x4{pk2(o[0] * rl, o[1] * rl), pk2(o[2] * rl, o[3] * rl), pk2(o[4] * rl, o[5] * rl), pk2(o[6] * rl, o[7] * rl)});
      if (h == 0) st8(rs, st + rw * 8, u32x2{pk2(o[8] * rl, o[9] * rl), pk2(o[10] * rl, o[11] * rl)});
      DRAIN();
      flag_set(ws, F_O + b * 8 + w, tag, lane);
      lds_arrive(3);
    }
    STAMP(16);
    FLUSH(0x1ffff);
  }
}

#define WS_SHIFT 0
extern "C" void kernel_launch(void* const* d_in, const int* in_sizes, int n_in, void* d_out, int out_size, void* d_ws, size_t ws_size,
                              hipStream_t stream) {
  (void)hipFuncSetAttribute((const void*)fused_kernel, hipFuncAttributeMaxDynamicSharedMemorySize, LDS_BYTES);
  fused_kernel<<<256, 768, LDS_BYTES, stream>>>((const unsigned short*)d_in[0], (const unsigned short*)d_in[1], (const unsigned short*)d_in[2],
                                                (const unsigned short*)d_in[4], (const unsigned short*)d_in[5], (const unsigned short*)d_in[6],
                                                (char*)d_ws + WS_SHIFT, (unsigned short*)d_out);
}
```
